```python
import torch

def _softmax(x, axis=-1):
    return torch.softmax(x, dim=axis)
def _gelu(x):
    return torch.nn.functional.gelu(x, approximate="tanh")
def _silu(x):
    return torch.nn.functional.silu(x)
def _sigmoid(x):
    return torch.sigmoid(x)
def _layernorm(x, axis=-1, eps=1e-5):
    m = x.mean(dim=axis, keepdim=True)
    v = x.var(dim=axis, keepdim=True, unbiased=False)
    return (x - m) / torch.sqrt(v + eps)
def _scatter_add(tgt, idx, src, axis=0):
    return tgt.clone().index_add_(axis, idx, src)
def _segment_sum(src, seg, n):
    out = torch.zeros((n,) + src.shape[1:], dtype=src.dtype)
    return out.index_add_(0, seg, src)
def _pad(x, pw):
    flat = tuple(v for pair in reversed(pw) for v in pair)
    return torch.nn.functional.pad(x, flat)

kernel_name = 'recurrent_scan__linear_kernel_hedgehog__mixer_channel_mlp__prompt'

def setup_inputs():

    _w = (lambda a: (lambda t: t.to(torch.bfloat16) if t.is_floating_point() else t)(torch.as_tensor(a)))
    def _A_setup():
        B, S, D, H = 1, 2048, 512, 32
        Dh = D // H
        return {
            "x": _w((lambda *s: torch.randn(*s, dtype=torch.bfloat16) * 0.05)(B, S, D)),
            "wqkv": _w((lambda *s: torch.randn(*s, dtype=torch.bfloat16) * 0.2)(D, 3 * D)),
            "w_phi": _w((lambda *s: torch.randn(*s, dtype=torch.bfloat16) * 0.2)(H, Dh, Dh) * (Dh ** -0.5)),
            "wo": _w((lambda *s: torch.randn(*s, dtype=torch.bfloat16) * 0.2)(D, D)),
        }
    _A_in = _A_setup()

    def _B_setup():
        B, S, D, F = 1, 2048, 512, 1376
        return {
            "x": _w((lambda *s: torch.randn(*s, dtype=torch.bfloat16) * 0.05)(B, S, D)),
            "w1": _w((lambda *s: torch.randn(*s, dtype=torch.bfloat16) * 0.8)(D, F) * (D ** -0.5)),
            "b1": _w((lambda *s: torch.randn(*s, dtype=torch.bfloat16) * 0.8)(F)),
            "w2": _w((lambda *s: torch.randn(*s, dtype=torch.bfloat16) * 0.1)(F, D) * (F ** -0.5)),
            "b2": _w((lambda *s: torch.randn(*s, dtype=torch.bfloat16) * 0.1)(D)),
        }
    _B_in = _B_setup()

    return {
        "A_x": _A_in["x"],
        "A_wqkv": _A_in["wqkv"],
        "A_w_phi": _A_in["w_phi"],
        "A_wo": _A_in["wo"],
        "B_x": _B_in["x"],
        "B_w1": _B_in["w1"],
        "B_b1": _B_in["b1"],
        "B_w2": _B_in["w2"],
        "B_b2": _B_in["b2"],
    }

def reference(A_x, A_wqkv, A_w_phi, A_wo, B_x, B_w1, B_b1, B_w2, B_b2):
    def _A_ref(x, wqkv, w_phi, wo):
        B, S, D = x.shape
        H = 32; Dh = D // H
        qkv = (lambda x, *d: x.permute(*d))((x @ wqkv).reshape(B, S, 3, H, Dh), 2, 0, 3, 1, 4)
        q, k, v = qkv[0], qkv[1], qkv[2]
        pq = torch.einsum("bhsd,hdf->bhsf", q, w_phi)
        pk = torch.einsum("bhsd,hdf->bhsf", k, w_phi)
        phi_q = torch.concatenate([_softmax(pq, axis=-1), _softmax(-pq, axis=-1)], axis=-1)
        phi_k = torch.concatenate([_softmax(pk, axis=-1), _softmax(-pk, axis=-1)], axis=-1)
        kv = torch.einsum("bhsf,bhsd->bhfd", phi_k, v)
        z  = torch.einsum("bhsf->bhf", phi_k)
        num = torch.einsum("bhqf,bhfd->bhqd", phi_q, kv)
        den = torch.einsum("bhqf,bhf->bhq", phi_q, z)[..., None] + 1e-6
        o = num / den
        return (lambda x, *d: x.permute(*d))(o, 0, 2, 1, 3).reshape(B, S, D) @ wo

    def _B_ref(x, w1, b1, w2, b2):
        y = _layernorm(x, axis=-1)
        h = _gelu(torch.einsum("bsd,df->bsf", y, w1) + b1)
        return torch.einsum("bsf,fd->bsd", h, w2) + b2

    x = A_x
    xn = _layernorm(x, axis=-1)
    s = _A_ref(xn, A_wqkv, A_w_phi, A_wo)
    g = _silu(_B_ref(xn, B_w1, B_b1, B_w2, B_b2))
    return x + s * g
```

```cpp
#include <hip/hip_runtime.h>
#include <hip/hip_bf16.h>
#pragma clang diagnostic ignored "-Wint-to-pointer-cast"

typedef unsigned u32;
typedef unsigned long long u64;
typedef u32 v4u __attribute__((ext_vector_type(4)));
typedef u32 v2u __attribute__((ext_vector_type(2)));
typedef float f2 __attribute__((ext_vector_type(2)));
typedef float f4 __attribute__((ext_vector_type(4)));
typedef float f16v __attribute__((ext_vector_type(16)));
typedef __bf16 bf2 __attribute__((ext_vector_type(2)));
typedef __bf16 bf8 __attribute__((ext_vector_type(8)));
typedef short s4 __attribute__((ext_vector_type(4)));
#define LAS __attribute__((address_space(3)))
#define DEV __device__ __forceinline__

#define FSTR 128u
#define WS_E 0u
#define WS_FKV 4096u
#define WS_FH (WS_FKV + 1024u * FSTR)
#define WS_FO (WS_FH + 1024u * FSTR)
#define WS_ZP (WS_FO + 1024u * FSTR)
#define WS_KVP (WS_ZP + 65536u)
#define WS_O (WS_KVP + 524288u)
#define WS_H (WS_O + 2097152u)
#define WS_END (WS_H + 64u * 86u * 1024u)
#define WS_STAMP (16u << 20)
#define SLOT(i) ((u32)(i) * 32768u)
#define SC_V 131072u
#define SC_STAT 147456u
#define SC_CS 151552u
#define SC_CS2 153600u
#define SM_WB 155648u
#define SM_B2 157696u
#define SM_KVT 158720u
#define SM_E 163072u
#define SM_DUMMY 163328u
#define LDS_BYTES 163840u
#define SPIN_CAP 60000

#define STAMP(i)

template <int N> struct IC { static constexpr int v = N; };
struct Params {
  const char* x; const char* wqkv; const char* wphi; const char* wo; const char* w1;
  const char* b1; const char* w2; const char* b2; char* out; char* ws;
};

DEV v4u mk_srd(const void* p, u32 bytes) {
  const u64 a = (u64)p;
  return (v4u){(u32)a, (u32)(a >> 32) & 0xffffu, bytes, 0x00020000u};
}
DEV void dma16(v4u srd, u32 voff, u32 soff, u32 dst) {
  asm volatile("s_mov_b32 m0, %0\n\ts_nop 0\n\tbuffer_load_dwordx4 %1, %2, %3 offen lds" :: "s"(dst), "v"(voff), "s"(srd), "s"(soff) : "memory");
}
DEV void dma4(v4u srd, u32 voff, u32 dst) {
  asm volatile("s_mov_b32 m0, %0\n\ts_nop 0\n\tbuffer_load_dword %1, %2, 0 offen lds" :: "s"(dst), "v"(voff), "s"(srd) : "memory");
}
DEV void dma4_sc1(v4u srd, u32 voff, u32 dst) {
  asm volatile("s_mov_b32 m0, %0\n\ts_nop 0\n\tbuffer_load_dword %1, %2, 0 offen sc1 lds" :: "s"(dst), "v"(voff), "s"(srd) : "memory");
}
#define WAIT_VM(n) asm volatile("s_waitcnt vmcnt(" #n ")" ::: "memory")
#define BAR_VM(n) asm volatile("s_waitcnt vmcnt(" #n ") lgkmcnt(0)\n\ts_barrier" ::: "memory")
#define BAR() asm volatile("s_waitcnt lgkmcnt(0)\n\ts_barrier" ::: "memory")
#define FENCE() __builtin_amdgcn_sched_barrier(0)

DEV v4u lds16(u32 a) { return *(LAS v4u*)a; }
DEV v2u ldstr(u32 a) { return __builtin_bit_cast(v2u, __builtin_amdgcn_ds_read_tr16_b64_v4i16((LAS s4*)a)); }
DEV v4u ldsfrag(u32 a, u32 d) { const v2u p = ldstr(a), q = ldstr(a + d); return (v4u){p.x, p.y, q.x, q.y}; }
DEV f16v mfma32(v4u a, v4u b, f16v c) { return __builtin_amdgcn_mfma_f32_32x32x16_bf16(__builtin_bit_cast(bf8, a), __builtin_bit_cast(bf8, b), c, 0, 0, 0); }
DEV f4 mfma16(v4u a, v4u b, f4 c) { return __builtin_amdgcn_mfma_f32_16x16x32_bf16(__builtin_bit_cast(bf8, a), __builtin_bit_cast(bf8, b), c, 0, 0, 0); }
DEV u32 pk(float a, float b) { return __builtin_bit_cast(u32, __builtin_convertvector((f2){a, b}, bf2)); }
DEV float blo(u32 v) { return __uint_as_float(v << 16); }
DEV float bhi(u32 v) { return __uint_as_float(v & 0xffff0000u); }
DEV v4u pk8(const f16v& a, int o) { return (v4u){pk(a[o], a[o + 1]), pk(a[o + 2], a[o + 3]), pk(a[o + 4], a[o + 5]), pk(a[o + 6], a[o + 7])}; }

DEV v4u fold_w(float cs, u32 b16) {
  const u32 h = pk(cs, cs) & 0xffffu;
  const u32 l = pk(cs - blo(h), 0.f);
  return (v4u){h | (l << 16), h | (b16 << 16), b16, 0u};
}
DEV void softmax16(const f16v& p, float c, v4u& o0, v4u& o1) {
  float mx = fmaxf(fmaxf(p[0], p[1]), p[2]);
#pragma unroll
  for (int i = 3; i < 15; i += 2) mx = fmaxf(fmaxf(mx, p[i]), p[i + 1]);
  mx = fmaxf(mx, p[15]);
  const float nm = -mx * c;
  f16v e; float s = 0.f;
#pragma unroll
  for (int i = 0; i < 16; ++i) { e[i] = __builtin_amdgcn_exp2f(__builtin_fmaf(p[i], c, nm)); s += e[i]; }
  const float rs = __builtin_amdgcn_rcpf(s);
#pragma unroll
  for (int i = 0; i < 16; ++i) e[i] *= rs;
  o0 = pk8(e, 0); o1 = pk8(e, 8);
}
DEV void gelu16(f16v& a, float c0, float c1, float sig) {
#pragma unroll
  for (int i = 0; i < 16; ++i) {
    const float t = a[i] * a[i];
    const float e = __builtin_amdgcn_exp2f(a[i] * __builtin_fmaf(t, c1, c0));
    a[i] = a[i] * __builtin_amdgcn_rcpf(__builtin_fmaf(e, sig, sig));
  }
}
DEV void wait_flags(__amdgpu_buffer_rsrc_t rs, u32 voff, u32 tag, u32 tag2) {
#pragma unroll 1
  for (int sp = 0; sp < SPIN_CAP; ++sp) {
    const v2u f = __builtin_amdgcn_raw_buffer_load_b64(rs, voff, 0, 16);
    if (__all(f.x == tag && f.y == tag2)) break;
    asm volatile("" ::: "memory");
  }
}

__global__ void __launch_bounds__(512, 2) hh_fused(Params p) {
  const u32 tid = threadIdx.x, lane = tid & 63u;
  const u32 w = __builtin_amdgcn_readfirstlane(tid >> 6);
  const u32 b = blockIdx.x;
  const u32 xcd = b & 7u, rb = b >> 3, xg = xcd >> 1, gl = rb >> 3;
  const u32 isM = gl & 1u;
  const u32 idx = xg * 2u + (gl >> 1);
  const u32 t = (xcd & 1u) * 8u + (rb & 7u);
  const u32 koff = ((rb & 7u) + 2u * gl) & 7u;
  const u32 tb = (xcd & 1u) * 16u + xg * 4u + (rb & 3u), nb = rb >> 2;
  const u32 r = w & 3u, hf = w >> 2;
  const u32 hh = lane >> 5, l31 = lane & 31u, q4 = lane & 3u, rr = (lane >> 2) & 3u, g1 = (lane >> 4) & 1u;

  const v4u srdWs = mk_srd(p.ws, WS_END);
  const u32 seg0 = idx <= 3u ? 6u * idx : 18u + 5u * (idx - 3u);
  const u32 six = idx < 3u;
  if (w < 4u) {
    u64 pc; u32 len;
    asm volatile("s_getpc_b64 %0\n.Lhh_pc:\n\ts_mov_b32 %1, .Lhh_end-.Lhh_pc" : "=s"(pc), "=s"(len));
    dma4(mk_srd((const void*)pc, len), w * 8192u + lane * 128u, SM_DUMMY);
  } else if (w < 6u) {
    if (isM) dma16(mk_srd(p.b1, 2752u), seg0 * 64u + lane * 16u, 0u, SM_WB + (w - 4u) * 1024u);
    else dma16(mk_srd(p.wphi, 16384u), idx * 2048u + (w - 4u) * 1024u + lane * 16u, 0u, SM_WB + (w - 4u) * 1024u);
  } else if (w == 6u) {
    dma16(mk_srd(p.b2, 1024u), lane * 16u, 0u, SM_B2);
  } else {
    dma4_sc1(srdWs, lane * 131072u + (lane ? 2048u : 0u), SM_E);
  }

  const u32 wstr = isM ? 2752u : 3072u;
  const v4u srdX = mk_srd(p.x, 2097152u);
  const v4u srdW = mk_srd(isM ? p.w1 : p.wqkv, 512u * wstr);
  const u32 row7 = (lane >> 2) & 7u;
  const u32 vx0 = (16u * w + row7) * 1024u + hh * 64u + 16u * (q4 ^ (row7 >> 2));
  const u32 vx1 = (vx0 + 8192u) ^ 32u;
  const u32 colb = isM ? seg0 * 64u : 1024u + idx * 128u;
  const u32 vw = (8u * w + rr) * wstr + colb + g1 * 64u + hh * (isM ? 128u : 1024u) + 16u * q4;
  const u32 xs = t * 131072u;
  auto item_chunk = [&](u32 cp) {
    const u32 kc = (cp + koff) & 7u, sl = SLOT(cp & 3u);
    dma16(srdX, vx0, xs + kc * 128u, sl + w * 2048u);
    dma16(srdW, vw, kc * 64u * wstr, sl + 16384u + w * 2048u);
    dma16(srdX, vx1, xs + kc * 128u, sl + w * 2048u + 1024u);
    dma16(srdW, vw, (kc * 64u + 4u) * wstr, sl + 16384u + w * 2048u + 1024u);
  };
  item_chunk(0); item_chunk(1); item_chunk(2);

  const u32 vu = (8u * w + 4u * hh + rr) * wstr + (isM ? seg0 * 64u + 256u + (six ? g1 * 64u : 0u) : idx * 128u + g1 * 64u) + 16u * q4;
  auto item_u2 = [&](u32 h2) {
#pragma unroll
    for (u32 e = 0; e < 4; ++e) dma16(srdW, vu, ((h2 * 4u + e + koff) & 7u) * 64u * wstr, SLOT(h2) + e * 8192u + w * 1024u);
  };
  const v4u srdW2 = mk_srd(p.w2, 1376u * 1024u);
  const v4u srdWo = mk_srd(p.wo, 512u * 1024u);
  const u32 vp = (4u * hh + rr) * 1024u + nb * 128u + g1 * 64u + 16u * q4;
  auto piece = [&](v4u srd, u32 row0, u32 dst, u32 n, u32 nw, u32 iw) {
#pragma unroll
    for (u32 e = 0; e < n; ++e) dma16(srd, vp, (row0 + (e * nw + iw) * 8u) * 1024u, dst + (e * nw + iw) * 1024u);
  };

  const u32 rot = r ^ (2u * hf);
  const u32 xa0 = (4u * r + (l31 >> 3)) * 1024u + (l31 & 7u) * 64u + ((16u * (hh ^ ((l31 >> 2) & 3u))) ^ ((rot & 1u) * 32u)) + (rot >> 1) * 512u;
  const u32 wl = hf * 256u + rr * 64u + g1 * 32u + q4 * 8u;
  const u32 wa0 = 16384u + rot * 4096u + hh * 2048u + wl;
  const u32 one2 = 0x3f803f80u;
  const u32 selv = ((lane & 15u) == (g1 ? 4u : 0u)) ? one2 : 0u;
  const v4u sel = {selv, selv, selv, selv};

  v4u xr[32];
  v4u wA[4], wB[4];
  f16v acc0, acc1;
  f4 cs0 = {0.f, 0.f, 0.f, 0.f}, cs1 = {0.f, 0.f, 0.f, 0.f};
#pragma unroll
  for (int i = 0; i < 16; ++i) { acc0[i] = 0.f; acc1[i] = 0.f; }
  float sum = 0.f, sq = 0.f;
  auto stats = [&](v4u v) {
#pragma unroll
    for (int i = 0; i < 4; ++i) {
      const float lo = blo(v[i]), hi = bhi(v[i]);
      sum += lo; sq = __builtin_fmaf(lo, lo, sq);
      sum += hi; sq = __builtin_fmaf(hi, hi, sq);
    }
    asm volatile("" : "+v"(sum), "+v"(sq));
  };
#pragma unroll
  for (u32 cp = 0; cp < 8; ++cp) {
    BAR_VM(8);
    if (cp == 0) STAMP(1);
    if (cp < 5) item_chunk(cp + 3);
    else if (cp < 7) item_u2(cp - 5);
    else {
      piece(srdWo, 0u, SLOT(2), 4, 8, w);
      const u32 c = (rb & 3u) * 8u + w;
      dma4(c < 22u ? srdW2 : srdWo, ((c < 22u ? c : c - 22u) * 64u + lane) * 1024u + nb * 128u, SM_DUMMY);
    }
    const u32 sl = SLOT(cp & 3u);
#pragma unroll
    for (u32 s = 0; s < 2; ++s) {
      xr[cp * 4 + s] = lds16(sl + (xa0 ^ (s * 32u)));
      wA[s * 2] = ldsfrag(sl + (wa0 ^ (s * 4096u)), 1024u);
      wA[s * 2 + 1] = ldsfrag(sl + (wa0 ^ (s * 4096u)) + 512u, 1024u);
    }
    FENCE();
    if (cp > 0) {
      acc0 = mfma32(wB[0], xr[cp * 4 - 2], acc0); acc1 = mfma32(wB[1], xr[cp * 4 - 2], acc1);
      acc0 = mfma32(wB[2], xr[cp * 4 - 1], acc0); acc1 = mfma32(wB[3], xr[cp * 4 - 1], acc1);
      stats(xr[cp * 4 - 3]);
    }
    FENCE();
#pragma unroll
    for (u32 s = 2; s < 4; ++s) {
      xr[cp * 4 + s] = lds16(sl + (xa0 ^ (512u + (s & 1u) * 32u)));
      wB[s * 2 - 4] = ldsfrag(sl + (wa0 ^ (s * 4096u)), 1024u);
      wB[s * 2 - 3] = ldsfrag(sl + (wa0 ^ (s * 4096u)) + 512u, 1024u);
    }
    FENCE();
    acc0 = mfma32(wA[0], xr[cp * 4], acc0); acc1 = mfma32(wA[1], xr[cp * 4], acc1);
    cs0 = mfma16(sel, wA[0], cs0); cs1 = mfma16(sel, wA[1], cs1);
    acc0 = mfma32(wA[2], xr[cp * 4 + 1], acc0); acc1 = mfma32(wA[3], xr[cp * 4 + 1], acc1);
    stats(xr[cp * 4]);
    FENCE();
  }
  acc0 = mfma32(wB[0], xr[30], acc0); acc1 = mfma32(wB[1], xr[30], acc1);
  acc0 = mfma32(wB[2], xr[31], acc0); acc1 = mfma32(wB[3], xr[31], acc1);
  stats(xr[29]);
  *(LAS f2*)(SC_STAT + (32u * r + l31) * 32u + (hf * 2u + hh) * 8u) = (f2){sum, sq};
  if (lane < 32u) {
    *(LAS float*)(SC_CS + (hf * 32u + lane) * 16u + r * 4u) = cs0[0];
    *(LAS float*)(SC_CS + 1024u + (hf * 32u + lane) * 16u + r * 4u) = cs1[0];
  }
  u32 tidv = tid;
  asm volatile("" : "+v"(tidv));
  auto tail = [&](auto ismc, auto hfc) {
  constexpr bool ISM = decltype(ismc)::v; constexpr u32 hf = decltype(hfc)::v;
  const u32 tid = tidv, lane = tid & 63u;
  const u32 hh = lane >> 5, l31 = lane & 31u, q4 = lane & 3u, rr = (lane >> 2) & 3u, g1 = (lane >> 4) & 1u;
  const u32 wl = hf * 256u + rr * 64u + g1 * 32u + q4 * 8u;
  const u32 selv = ((lane & 15u) == (g1 ? 4u : 0u)) ? one2 : 0u;
  const v4u sel = {selv, selv, selv, selv};
  const u32 vp = (4u * hh + rr) * 1024u + nb * 128u + g1 * 64u + 16u * q4;
  auto piece = [&](v4u srd, u32 row0, u32 dst, u32 n, u32 nw, u32 iw) {
#pragma unroll
    for (u32 e = 0; e < n; ++e) dma16(srd, vp, (row0 + (e * nw + iw) * 8u) * 1024u, dst + (e * nw + iw) * 1024u);
  };
  if constexpr (ISM) { BAR_VM(5); } else { BAR(); }
  STAMP(2);

  const u32 ep = __builtin_amdgcn_readfirstlane(*(LAS u32*)SM_E);
  const u32 tag = ep * 1664525u + 1013904223u, tag2 = (ep ^ 0x9e3779b9u) * 2654435761u + 12345u;
  const __amdgpu_buffer_rsrc_t rsW = __builtin_amdgcn_make_buffer_rsrc(p.ws, 0, WS_END, 0x00020000);
  auto put_flag = [&](u32 off) {
    __builtin_amdgcn_raw_buffer_store_b64((v2u){tag, tag2}, rsW, lane == 0u ? off : 0x80000000u, 0, 16);
  };

  float mu, var;
  {
    const f4 a = *(LAS f4*)(SC_STAT + (32u * r + l31) * 32u), c = *(LAS f4*)(SC_STAT + (32u * r + l31) * 32u + 16u);
    mu = ((a[0] + a[2]) + (c[0] + c[2])) * (1.f / 512.f);
    var = fmaxf(((a[1] + a[3]) + (c[1] + c[3])) * (1.f / 512.f) - mu * mu, 0.f);
  }
  const float sig2 = ISM ? __builtin_fmaf(1e-5f, var + 1e-5f, var) : var + 1e-5f;
  const float rinv = __builtin_amdgcn_rsqf(sig2);
  const float sig = sig2 * rinv;
  v4u fx;
  {
    const u32 mh = pk(-mu, -mu);
    const u32 sh = pk(-mu - blo(mh), sig);
    const u32 sl_ = pk(sig - bhi(sh), 0.f);
    fx = (v4u){hh ? 0u : mh, hh ? 0u : sh, hh ? 0u : sl_, 0u};
  }
  auto cs_total = [&](u32 a) { const f4 c = *(LAS f4*)(a + (hf * 32u + l31) * 16u); return (c[0] + c[1]) + (c[2] + c[3]); };
  const u32 ua0 = rot * 2048u + hh * 1024u + wl;
  f16v acc2;
  f4 cs2 = {0.f, 0.f, 0.f, 0.f};
  auto g2 = [&](int c0, int c1) {
#pragma unroll
    for (int cp = c0; cp < c1; ++cp) {
      v4u u[4];
#pragma unroll
      for (u32 s = 0; s < 4; ++s) u[s] = ldsfrag(cp * 8192u + (ua0 ^ (s * 2048u)), 512u);
      cs2 = mfma16(sel, u[0], cs2);
#pragma unroll
      for (u32 s = 0; s < 4; ++s) acc2 = mfma32(u[s], xr[cp * 4 + s], acc2);
    }
  };
#pragma unroll
  for (int i = 0; i < 16; ++i) acc2[i] = 0.f;

  const u32 mrow = w & 1u, kh = (w >> 1) & 1u;
  const u32 pa0 = hh * 512u + rr * 64u + g1 * 32u + q4 * 8u;
  const u32 tok32 = 4u * t + r;
  v4u wr[32];
  auto to_regs = [&]() {
    if constexpr (hf) {
#pragma unroll
      for (u32 i = 0; i < 16; ++i) {
        const u32 a = SLOT(2) + kh * 32768u + ((i + 2u * nb) & 15u) * 2048u + pa0;
        wr[2 * i] = ldsfrag(a, 1024u); wr[2 * i + 1] = ldsfrag(a + 256u, 1024u);
      }
    } else {
#pragma unroll
      for (u32 i = 0; i < 5; ++i) {
        const u32 a = SC_V + (kh * 5u + i) * 2048u + pa0;
        wr[2 * i] = ldsfrag(a, 1024u); wr[2 * i + 1] = ldsfrag(a + 256u, 1024u);
      }
    }
  };

  if constexpr (ISM) {
    const float cA = cs_total(SC_CS), cB = cs_total(SC_CS + 1024u);
    const u32 bA = *(LAS unsigned short*)(SM_WB + hf * 64u + l31 * 2u), bB = *(LAS unsigned short*)(SM_WB + 128u + hf * 64u + l31 * 2u);
    const u32 bC = *(LAS unsigned short*)(SM_WB + 256u + (six ? hf * 64u : 0u) + l31 * 2u);
    BAR();
    if constexpr (hf) { piece(srdWo, 256u, SLOT(3), 8, 4, r); piece(srdW2, 1216u, SC_V, 5, 4, r); }
    acc0 = mfma32(fold_w(cA, bA), fx, acc0);
    acc1 = mfma32(fold_w(cB, bB), fx, acc1);
    const float c0 = -2.302208198f * rinv;
    const float c1 = -0.1029432397f * rinv * rinv * rinv;
    const u32 hb = WS_H + (tok32 * 86u + seg0 * 2u) * 1024u + lane * 16u;
    if constexpr (hf) g2(0, 8);
    gelu16(acc0, c0, c1, sig);
    __builtin_amdgcn_raw_buffer_store_b128(pk8(acc0, 0), rsW, hb + hf * 2048u, 0, 16);
    __builtin_amdgcn_raw_buffer_store_b128(pk8(acc0, 8), rsW, hb + hf * 2048u + 1024u, 0, 16);
    gelu16(acc1, c0, c1, sig);
    __builtin_amdgcn_raw_buffer_store_b128(pk8(acc1, 0), rsW, hb + hf * 2048u + 4096u, 0, 16);
    __builtin_amdgcn_raw_buffer_store_b128(pk8(acc1, 8), rsW, hb + hf * 2048u + 5120u, 0, 16);
    if (!hf && six) g2(0, 8);
    if (lane < 32u) *(LAS float*)(SC_CS2 + (hf * 32u + lane) * 16u + r * 4u) = cs2[0];
    BAR_VM(4);
    STAMP(5);
    if constexpr (hf) piece(srdW2, 0u, SLOT(0), 16, 4, r);
    const float cC = cs_total(SC_CS2);
    to_regs();
    if (hf || six) {
      acc2 = mfma32(fold_w(cC, bC), fx, acc2);
      gelu16(acc2, c0, c1, sig);
      const u32 h2 = hb + 8192u + (six ? hf * 2048u : 0u);
      __builtin_amdgcn_raw_buffer_store_b128(pk8(acc2, 0), rsW, h2, 0, 16);
      __builtin_amdgcn_raw_buffer_store_b128(pk8(acc2, 8), rsW, h2 + 1024u, 0, 16);
    }
    BAR();
    STAMP(6);
    if constexpr (hf) { piece(srdW2, 512u, SLOT(2), 22, 4, r); WAIT_VM(22); } else { WAIT_VM(0); }
    put_flag(WS_FH + (tok32 * 16u + idx * 2u + hf) * FSTR);
    STAMP(9);
  } else {
    const float cA = cs_total(SC_CS), cB = cs_total(SC_CS + 1024u);
    v4u aphi[2];
    const u32 sgn = (lane & 4u) ? 0x80008000u : 0u;
#pragma unroll
    for (u32 e = 0; e < 2; ++e) {
      aphi[e] = ldsfrag(SM_WB + (2u * hf + e) * 512u + (4u * hh + rr) * 32u + (2u * g1 + (q4 >> 1)) * 8u, 256u);
      aphi[e] ^= (v4u){sgn, sgn, sgn, sgn};
    }
    acc0 = mfma32(fold_w(cA, 0u), fx, acc0);
    acc1 = mfma32(fold_w(cB, 0u), fx, acc1);
    const float cl = rinv * 1.4426950409f;
    f16v zero;
#pragma unroll
    for (int i = 0; i < 16; ++i) zero[i] = 0.f;
    const u32 tokl = 32u * r + l31, sw = (l31 >> 2) & 1u;
#pragma unroll
    for (u32 e = 0; e < 2; ++e) {
      const f16v pp = mfma32(aphi[e], pk8(acc0, 8 * e), zero);
      v4u o0, o1; softmax16(pp, cl, o0, o1);
      const u32 a = SLOT(3) + ((2u * hf + e) * 2u + hh) * 4096u + tokl * 32u;
      *(LAS v4u*)(a + sw * 16u) = o0; *(LAS v4u*)(a + (sw ^ 1u) * 16u) = o1;
    }
#pragma unroll
    for (int i = 0; i < 16; ++i) acc1[i] *= rinv;
#pragma unroll
    for (u32 e = 0; e < 2; ++e) *(LAS v4u*)(SC_V + (2u * hf + e) * 4096u + tokl * 32u + (hh ^ sw) * 16u) = pk8(acc1, 8 * e);
    BAR_VM(5);
    STAMP(3);
    {
      const u32 la = (16u * hh + 4u * g1 + rr) * 32u + ((q4 * 8u) ^ (g1 * 16u));
      f4 akv = {0.f, 0.f, 0.f, 0.f}, az = {0.f, 0.f, 0.f, 0.f};
      const v4u ones = {one2, one2, one2, one2};
#pragma unroll
      for (u32 ks = 0; ks < 4; ++ks) {
        const v4u fa = ldsfrag(SLOT(3) + (r * 2u + hf) * 4096u + ks * 1024u + la, 256u);
        const v4u fb = ldsfrag(SC_V + r * 4096u + ks * 1024u + la, 256u);
        akv = mfma16(fa, fb, akv); az = mfma16(fa, ones, az);
      }
      const u32 pi = (idx * 8u + w) * 16u + t;
      __builtin_amdgcn_raw_buffer_store_b64((v2u){pk(akv[0], akv[1]), pk(akv[2], akv[3])}, rsW, WS_KVP + pi * 512u + lane * 8u, 0, 16);
      __builtin_amdgcn_raw_buffer_store_b128(__builtin_bit_cast(v4u, az), rsW, (lane & 15u) == 0u ? WS_ZP + pi * 64u + (lane >> 4) * 16u : 0x80000000u, 0, 16);
      BAR();
      STAMP(4);
      if constexpr (hf) { piece(srdWo, 256u, SLOT(3), 8, 4, r); piece(srdW2, 1216u, SC_V, 5, 4, r); }
      g2(0, 4);
      if constexpr (hf) { WAIT_VM(13); } else { WAIT_VM(0); }
      put_flag(WS_FKV + pi * FSTR);
    }
    g2(4, 8);
    if (lane < 32u) *(LAS float*)(SC_CS2 + (hf * 32u + lane) * 16u + r * 4u) = cs2[0];
    BAR_VM(1);
    STAMP(5);
    if constexpr (hf) piece(srdW2, 0u, SLOT(0), 16, 4, r);
    const float cC = cs_total(SC_CS2);
    to_regs();
    acc2 = mfma32(fold_w(cC, 0u), fx, acc2);
    v4u phq[4];
#pragma unroll
    for (u32 e = 0; e < 2; ++e) {
      const f16v pp = mfma32(aphi[e], pk8(acc2, 8 * e), zero);
      softmax16(pp, cl, phq[2 * e], phq[2 * e + 1]);
    }
    STAMP(6);
    wait_flags(rsW, WS_FKV + ((idx * 8u + w) * 16u + (lane & 15u)) * FSTR, tag, tag2);
    STAMP(7);
    {
      v4u pv[8];
#pragma unroll
      for (u32 i = 0; i < 8; ++i) pv[i] = __builtin_amdgcn_raw_buffer_load_b128(rsW, WS_KVP + (idx * 8u + w) * 8192u + lane * 16u + i * 1024u, 0, 16);
      f4 z = __builtin_bit_cast(f4, __builtin_amdgcn_raw_buffer_load_b128(rsW, WS_ZP + (idx * 8u + w) * 1024u + lane * 16u, 0, 16));
      float s8[8];
#pragma unroll
      for (int j = 0; j < 4; ++j) { s8[2 * j] = blo(pv[0][j]); s8[2 * j + 1] = bhi(pv[0][j]); }
#pragma unroll
      for (int i = 1; i < 8; ++i)
#pragma unroll
        for (int j = 0; j < 4; ++j) { s8[2 * j] += blo(pv[i][j]); s8[2 * j + 1] += bhi(pv[i][j]); }
      float tt[4];
#pragma unroll
      for (int j = 0; j < 4; ++j) {
        const v2u sw2 = __builtin_amdgcn_permlane32_swap(__float_as_uint(s8[j]), __float_as_uint(s8[4 + j]), false, false);
        tt[j] = __uint_as_float(sw2.x) + __uint_as_float(sw2.y);
      }
      const u32 c = ((2u * l31) & 15u) + hh, d = (c & 3u) + 8u * ((c >> 2) & 1u) + 4u * (c >> 3);
      *(LAS v2u*)(SM_KVT + w * 544u + d * 32u + (l31 >> 3) * 8u) = (v2u){pk(tt[0], tt[1]), pk(tt[2], tt[3])};
#pragma unroll
      for (int j = 0; j < 4; ++j) {
        float v = z[j];
        v += __uint_as_float(__builtin_amdgcn_update_dpp(0u, __float_as_uint(v), 0x124, 0xf, 0xf, false));
        v += __uint_as_float(__builtin_amdgcn_update_dpp(0u, __float_as_uint(v), 0x128, 0xf, 0xf, false));
        v2u s2 = __builtin_amdgcn_permlane16_swap(__float_as_uint(v), __float_as_uint(v), false, false);
        v = __uint_as_float(s2.x) + __uint_as_float(s2.y);
        s2 = __builtin_amdgcn_permlane32_swap(__float_as_uint(v), __float_as_uint(v), false, false);
        z[j] = __uint_as_float(s2.x) + __uint_as_float(s2.y);
      }
      if (lane < 4u) *(LAS v2u*)(SM_KVT + w * 544u + 512u + lane * 8u) = (v2u){pk(z[0], z[1]), pk(z[2], z[3])};
    }
    BAR();
    STAMP(8);
#pragma unroll
    for (u32 e = 0; e < 2; ++e) {
      const u32 a = SM_KVT + (2u * hf + e + 4u * hh) * 544u + (l31 < 16u ? l31 : 16u) * 32u;
      f16v d = mfma32(lds16(a), phq[2 * e], zero);
      d = mfma32(lds16(a + 16u), phq[2 * e + 1], d);
      const float rd = __builtin_amdgcn_rcpf(d[8] + 1e-6f);
#pragma unroll
      for (int i = 0; i < 8; ++i) d[i] *= rd;
      __builtin_amdgcn_raw_buffer_store_b128(pk8(d, 0), rsW, WS_O + (tok32 * 32u + idx * 4u + 2u * hf + e) * 1024u + lane * 16u, 0, 16);
    }
    if constexpr (hf) { piece(srdW2, 512u, SLOT(2), 22, 4, r); WAIT_VM(22); } else { WAIT_VM(0); }
    put_flag(WS_FO + (tok32 * 16u + idx * 2u + hf) * FSTR);
    STAMP(9);
  }

  const u32 tk = 2u * tb + mrow;
  f16v d0, d1;
#pragma unroll
  for (int i = 0; i < 16; ++i) { d0[i] = 0.f; d1[i] = 0.f; }
  v4u xres;
  const u32 xo = (64u * tb + (tid >> 3)) * 1024u + nb * 128u + (tid & 7u) * 16u;
  if constexpr (!hf) {
    constexpr int WIN = 24;
    wait_flags(rsW, WS_FH + (tk * 16u + (lane & 15u)) * FSTR, tag, tag2);
    STAMP(10);
    const u32 hv = WS_H + tk * 86u * 1024u + lane * 16u;
    const u32 st0 = nb * 5u;
    auto kstep = [&](int i) -> u32 {
      if (i < 5) return 76u + kh * 5u + i;
      u32 k = st0 + (i - 5); k = k >= 38u ? k - 38u : k;
      return kh * 38u + k;
    };
    v4u act[43];
#pragma unroll
    for (int i = 0; i < WIN; ++i) act[i] = __builtin_amdgcn_raw_buffer_load_b128(rsW, hv, kstep(i) * 1024u, 16);
    xres = *(const v4u*)(p.x + xo);
    FENCE();
    BAR();
    STAMP(11);
    v4u n0, n1;
#pragma unroll
    for (int i = 0; i < 43; ++i) {
      v4u c0, c1;
      if (i < 5) { c0 = wr[2 * i]; c1 = wr[2 * i + 1]; } else { c0 = n0; c1 = n1; }
      if (i + 1 >= 5 && i + 1 < 43) { const u32 a = kstep(i + 1) * 2048u + pa0; n0 = ldsfrag(a, 1024u); n1 = ldsfrag(a + 256u, 1024u); }
      FENCE();
      d0 = mfma32(c0, act[i], d0); d1 = mfma32(c1, act[i], d1);
      if (i + WIN < 43) act[i + WIN] = __builtin_amdgcn_raw_buffer_load_b128(rsW, hv, kstep(i + WIN) * 1024u, 16);
      FENCE();
    }
  } else {
    BAR_VM(0);
    STAMP(11);
    xres = *(const v4u*)(p.x + xo);
    if (b == 0u && w == 7u) {
      wait_flags(rsW, WS_FH + ((lane >> 3) * 64u + (lane & 7u) * 2u) * FSTR, tag, tag2);
      wait_flags(rsW, WS_FH + ((lane >> 3) * 64u + 512u + (lane & 7u) * 2u) * FSTR, tag, tag2);
    }
    wait_flags(rsW, WS_FO + (tk * 16u + kh * 8u + (lane & 7u)) * FSTR, tag, tag2);
    STAMP(10);
    const u32 ov = WS_O + (tk * 32u + kh * 16u) * 1024u + lane * 16u;
    v4u act[16];
#pragma unroll
    for (u32 i = 0; i < 16; ++i) act[i] = __builtin_amdgcn_raw_buffer_load_b128(rsW, ov, ((i + 2u * nb) & 15u) * 1024u, 16);
    FENCE();
#pragma unroll
    for (u32 i = 0; i < 16; ++i) { d0 = mfma32(wr[2 * i], act[i], d0); d1 = mfma32(wr[2 * i + 1], act[i], d1); }
  }
  STAMP(12);
  BAR();
  if (b == 0u && w == 7u) __builtin_amdgcn_raw_buffer_store_b32(tag, rsW, lane == 0u ? WS_E : 0x80000000u, 0, 16);
  {
    const u32 a = (hf * 2u + kh) * 17408u + (32u * mrow + l31) * 272u + hh * 16u;
#pragma unroll
    for (int g = 0; g < 4; ++g) {
      *(LAS f4*)(a + g * 32u) = (f4){d0[4 * g], d0[4 * g + 1], d0[4 * g + 2], d0[4 * g + 3]};
      *(LAS f4*)(a + 128u + g * 32u) = (f4){d1[4 * g], d1[4 * g + 1], d1[4 * g + 2], d1[4 * g + 3]};
    }
  }
  BAR();
  STAMP(13);
  {
    const u32 a = (tid >> 3) * 272u + (tid & 7u) * 32u;
    const v4u bb = lds16(SM_B2 + nb * 128u + (tid & 7u) * 16u);
    float res[8];
#pragma unroll
    for (int hx = 0; hx < 2; ++hx) {
      const f4 m0 = *(LAS f4*)(a + hx * 16u), m1 = *(LAS f4*)(a + 17408u + hx * 16u);
      const f4 s0 = *(LAS f4*)(a + 34816u + hx * 16u), s1 = *(LAS f4*)(a + 52224u + hx * 16u);
#pragma unroll
      for (int j = 0; j < 4; ++j) {
        const u32 bw = bb[hx * 2 + (j >> 1)], xw = xres[hx * 2 + (j >> 1)];
        const float m = (m0[j] + m1[j]) + ((j & 1) ? bhi(bw) : blo(bw));
        const float g = m * __builtin_amdgcn_rcpf(1.f + __builtin_amdgcn_exp2f(m * -1.4426950409f));
        res[hx * 4 + j] = __builtin_fmaf(s0[j] + s1[j], g, (j & 1) ? bhi(xw) : blo(xw));
      }
    }
    *(v4u*)(p.out + xo) = (v4u){pk(res[0], res[1]), pk(res[2], res[3]), pk(res[4], res[5]), pk(res[6], res[7])};
  }
  };
  auto finish = [&]() {
    asm volatile("s_endpgm" : "=v"(xr[0]), "=v"(xr[1]), "=v"(xr[2]), "=v"(xr[3]), "=v"(xr[4]), "=v"(xr[5]), "=v"(xr[6]), "=v"(xr[7]),
                 "=v"(xr[8]), "=v"(xr[9]), "=v"(xr[10]), "=v"(xr[11]), "=v"(xr[12]), "=v"(xr[13]), "=v"(xr[14]), "=v"(xr[15]), "=v"(acc0), "=v"(acc1) :: "memory");
    asm volatile("" : "=v"(xr[16]), "=v"(xr[17]), "=v"(xr[18]), "=v"(xr[19]), "=v"(xr[20]), "=v"(xr[21]), "=v"(xr[22]), "=v"(xr[23]),
                 "=v"(xr[24]), "=v"(xr[25]), "=v"(xr[26]), "=v"(xr[27]), "=v"(xr[28]), "=v"(xr[29]), "=v"(xr[30]), "=v"(xr[31]), "=v"(tidv) :: "memory");
  };
  u32 role = isM * 2u + hf;
  if (role == 0u) { tail(IC<0>{}, IC<0>{}); finish(); }
  asm volatile("" : "+s"(role));
  if (role == 1u) { tail(IC<0>{}, IC<1>{}); finish(); }
  asm volatile("" : "+s"(role));
  if (role == 2u) { tail(IC<1>{}, IC<0>{}); finish(); }
  tail(IC<1>{}, IC<1>{});
  asm volatile(".Lhh_end:");
}

extern "C" void kernel_launch(void* const* d_in, const int* in_sizes, int n_in,
                              void* d_out, int out_size, void* d_ws, size_t ws_size,
                              hipStream_t stream) {
  Params p;
  p.x = (const char*)d_in[0]; p.wqkv = (const char*)d_in[1]; p.wphi = (const char*)d_in[2]; p.wo = (const char*)d_in[3];
  p.w1 = (const char*)d_in[5]; p.b1 = (const char*)d_in[6]; p.w2 = (const char*)d_in[7]; p.b2 = (const char*)d_in[8];
  p.out = (char*)d_out; p.ws = (char*)d_ws;
  (void)hipFuncSetAttribute((const void*)hh_fused, hipFuncAttributeMaxDynamicSharedMemorySize, (int)LDS_BYTES);
  hh_fused<<<256, 512, LDS_BYTES, stream>>>(p);
}
```

```cpp
#define PADSTAGE 2
#define PADT 2500ull
#include <hip/hip_runtime.h>
#include <hip/hip_bf16.h>
#pragma clang diagnostic ignored "-Wint-to-pointer-cast"

typedef unsigned u32;
typedef unsigned long long u64;
typedef u32 v4u __attribute__((ext_vector_type(4)));
typedef u32 v2u __attribute__((ext_vector_type(2)));
typedef float f2 __attribute__((ext_vector_type(2)));
typedef float f4 __attribute__((ext_vector_type(4)));
typedef float f16v __attribute__((ext_vector_type(16)));
typedef __bf16 bf2 __attribute__((ext_vector_type(2)));
typedef __bf16 bf8 __attribute__((ext_vector_type(8)));
typedef short s4 __attribute__((ext_vector_type(4)));
#define LAS __attribute__((address_space(3)))
#define DEV __device__ __forceinline__

#define FSTR 128u
#define WS_E 0u
#define WS_FKV 4096u
#define WS_FH (WS_FKV + 1024u * FSTR)
#define WS_FO (WS_FH + 1024u * FSTR)
#define WS_ZP (WS_FO + 1024u * FSTR)
#define WS_KVP (WS_ZP + 65536u)
#define WS_O (WS_KVP + 524288u)
#define WS_H (WS_O + 2097152u)
#define WS_END (WS_H + 64u * 86u * 1024u)
#define WS_STAMP (16u << 20)
#define SLOT(i) ((u32)(i) * 32768u)
#define SC_V 131072u
#define SC_STAT 147456u
#define SC_CS 151552u
#define SC_CS2 153600u
#define SM_WB 155648u
#define SM_B2 157696u
#define SM_KVT 158720u
#define SM_E 163072u
#define SM_DUMMY 163328u
#define LDS_BYTES 163840u
#define SPIN_CAP 60000

#define STAMP(i) do { if ((i) == PADSTAGE) { for (int sp_ = 0; sp_ < 100000; ++sp_) { u64 t_; asm volatile("s_memrealtime %0\n\ts_waitcnt lgkmcnt(0)" : "=s"(t_) :: "memory"); if (t_ - rt0 >= PADT) break; } } } while (0)

template <int N> struct IC { static constexpr int v = N; };
struct Params {
  const char* x; const char* wqkv; const char* wphi; const char* wo; const char* w1;
  const char* b1; const char* w2; const char* b2; char* out; char* ws;
};

DEV v4u mk_srd(const void* p, u32 bytes) {
  const u64 a = (u64)p;
  return (v4u){(u32)a, (u32)(a >> 32) & 0xffffu, bytes, 0x00020000u};
}
DEV void dma16(v4u srd, u32 voff, u32 soff, u32 dst) {
  asm volatile("s_mov_b32 m0, %0\n\ts_nop 0\n\tbuffer_load_dwordx4 %1, %2, %3 offen lds" :: "s"(dst), "v"(voff), "s"(srd), "s"(soff) : "memory");
}
DEV void dma4(v4u srd, u32 voff, u32 dst) {
  asm volatile("s_mov_b32 m0, %0\n\ts_nop 0\n\tbuffer_load_dword %1, %2, 0 offen lds" :: "s"(dst), "v"(voff), "s"(srd) : "memory");
}
DEV void dma4_sc1(v4u srd, u32 voff, u32 dst) {
  asm volatile("s_mov_b32 m0, %0\n\ts_nop 0\n\tbuffer_load_dword %1, %2, 0 offen sc1 lds" :: "s"(dst), "v"(voff), "s"(srd) : "memory");
}
#define WAIT_VM(n) asm volatile("s_waitcnt vmcnt(" #n ")" ::: "memory")
#define BAR_VM(n) asm volatile("s_waitcnt vmcnt(" #n ") lgkmcnt(0)\n\ts_barrier" ::: "memory")
#define BAR() asm volatile("s_waitcnt lgkmcnt(0)\n\ts_barrier" ::: "memory")
#define FENCE() __builtin_amdgcn_sched_barrier(0)

DEV v4u lds16(u32 a) { return *(LAS v4u*)a; }
DEV v2u ldstr(u32 a) { return __builtin_bit_cast(v2u, __builtin_amdgcn_ds_read_tr16_b64_v4i16((LAS s4*)a)); }
DEV v4u ldsfrag(u32 a, u32 d) { const v2u p = ldstr(a), q = ldstr(a + d); return (v4u){p.x, p.y, q.x, q.y}; }
DEV f16v mfma32(v4u a, v4u b, f16v c) {
  return __builtin_amdgcn_mfma_f32_32x32x16_bf16(__builtin_bit_cast(bf8, a), __builtin_bit_cast(bf8, b), c, 0, 0, 0); }
DEV f4 mfma16(v4u a, v4u b, f4 c) { return __builtin_amdgcn_mfma_f32_16x16x32_bf16(__builtin_bit_cast(bf8, a), __builtin_bit_cast(bf8, b), c, 0, 0, 0); }
DEV u32 pk(float a, float b) { return __builtin_bit_cast(u32, __builtin_convertvector((f2){a, b}, bf2)); }
DEV float blo(u32 v) { return __uint_as_float(v << 16); }
DEV float bhi(u32 v) { return __uint_as_float(v & 0xffff0000u); }
DEV v4u pk8(const f16v& a, int o) { return (v4u){pk(a[o], a[o + 1]), pk(a[o + 2], a[o + 3]), pk(a[o + 4], a[o + 5]), pk(a[o + 6], a[o + 7])}; }

DEV v4u fold_w(float cs, u32 b16) {
  const u32 h = pk(cs, cs) & 0xffffu;
  const u32 l = pk(cs - blo(h), 0.f);
  return (v4u){h | (l << 16), h | (b16 << 16), b16, 0u};
}
DEV void softmax16(const f16v& p, float c, v4u& o0, v4u& o1) {
  float mx = fmaxf(fmaxf(p[0], p[1]), p[2]);
#pragma unroll
  for (int i = 3; i < 15; i += 2) mx = fmaxf(fmaxf(mx, p[i]), p[i + 1]);
  mx = fmaxf(mx, p[15]);
  const float nm = -mx * c;
  f16v e; float s = 0.f;
#pragma unroll
  for (int i = 0; i < 16; ++i) { e[i] = __builtin_amdgcn_exp2f(__builtin_fmaf(p[i], c, nm)); s += e[i]; }
  const float rs = __builtin_amdgcn_rcpf(s);
#pragma unroll
  for (int i = 0; i < 16; ++i) e[i] *= rs;
  o0 = pk8(e, 0); o1 = pk8(e, 8);
}
DEV void gelu16(f16v& a, float c0, float c1, float sig) {
#pragma unroll
  for (int i = 0; i < 16; ++i) {
    const float t = a[i] * a[i];
    const float e = __builtin_amdgcn_exp2f(a[i] * __builtin_fmaf(t, c1, c0));
    a[i] = a[i] * __builtin_amdgcn_rcpf(__builtin_fmaf(e, sig, sig));
  }
}
DEV void wait_flags(__amdgpu_buffer_rsrc_t rs, u32 voff, u32 tag, u32 tag2) {
#pragma unroll 1
  for (int sp = 0; sp < SPIN_CAP; ++sp) {
    const v2u f = __builtin_amdgcn_raw_buffer_load_b64(rs, voff, 0, 16);
    if (__all(f.x == tag && f.y == tag2)) break;
    asm volatile("" ::: "memory");
  }
}

__global__ void __launch_bounds__(512, 2) hh_fused(Params p) {
  const u32 tid = threadIdx.x, lane = tid & 63u;
  const u32 w = __builtin_amdgcn_readfirstlane(tid >> 6);
  const u32 b = blockIdx.x;
  u64 rt0; asm volatile("s_memrealtime %0\n\ts_waitcnt lgkmcnt(0)" : "=s"(rt0) :: "memory");
  const u32 xcd = b & 7u, rb = b >> 3, xg = xcd >> 1, gl = rb >> 3;
  const u32 isM = gl & 1u;
  const u32 idx = xg * 2u + (gl >> 1);
  const u32 t = (xcd & 1u) * 8u + (rb & 7u);
  const u32 koff = ((rb & 7u) + 2u * gl) & 7u;
  const u32 tb = (xcd & 1u) * 16u + xg * 4u + (rb & 3u), nb = rb >> 2;
  const u32 r = w & 3u, hf = w >> 2;
  const u32 hh = lane >> 5, l31 = lane & 31u, q4 = lane & 3u, rr = (lane >> 2) & 3u, g1 = (lane >> 4) & 1u;

  const v4u srdWs = mk_srd(p.ws, WS_END);
  const u32 seg0 = idx <= 3u ? 6u * idx : 18u + 5u * (idx - 3u);
  const u32 six = idx < 3u;
  if (w < 4u) {
    u64 pc; u32 len;
    asm volatile("s_getpc_b64 %0\n.Lhh_pc:\n\ts_mov_b32 %1, .Lhh_end-.Lhh_pc" : "=s"(pc), "=s"(len));
    dma4(mk_srd((const void*)pc, len), w * 8192u + lane * 128u, SM_DUMMY);
  } else if (w < 6u) {
    if (isM) dma16(mk_srd(p.b1, 2752u), seg0 * 64u + lane * 16u, 0u, SM_WB + (w - 4u) * 1024u);
    else dma16(mk_srd(p.wphi, 16384u), idx * 2048u + (w - 4u) * 1024u + lane * 16u, 0u, SM_WB + (w - 4u) * 1024u);
  } else if (w == 6u) {
    dma16(mk_srd(p.b2, 1024u), lane * 16u, 0u, SM_B2);
  } else {
    dma4_sc1(srdWs, lane * 131072u + (lane ? 2048u : 0u), SM_E);
  }

  const u32 wstr = isM ? 2752u : 3072u;
  const v4u srdX = mk_srd(p.x, 2097152u);
  const v4u srdW = mk_srd(isM ? p.w1 : p.wqkv, 512u * wstr);
  const u32 row7 = (lane >> 2) & 7u;
  const u32 vx0 = (16u * w + row7) * 1024u + hh * 64u + 16u * (q4 ^ (row7 >> 2));
  const u32 vx1 = (vx0 + 8192u) ^ 32u;
  const u32 colb = isM ? seg0 * 64u : 1024u + idx * 128u;
  const u32 vw = (8u * w + rr) * wstr + colb + g1 * 64u + hh * (isM ? 128u : 1024u) + 16u * q4;
  const u32 xs = t * 131072u;
  auto item_chunk = [&](u32 cp) {
    const u32 kc = (cp + koff) & 7u, sl = SLOT(cp & 3u);
    dma16(srdX, vx0, xs + kc * 128u, sl + w * 2048u);
    dma16(srdW, vw, kc * 64u * wstr, sl + 16384u + w * 2048u);
    dma16(srdX, vx1, xs + kc * 128u, sl + w * 2048u + 1024u);
    dma16(srdW, vw, (kc * 64u + 4u) * wstr, sl + 16384u + w * 2048u + 1024u);
  };
  item_chunk(0); item_chunk(1); item_chunk(2);

  const u32 vu = (8u * w + 4u * hh + rr) * wstr + (isM ? seg0 * 64u + 256u + (six ? g1 * 64u : 0u) : idx * 128u + g1 * 64u) + 16u * q4;
  auto item_u2 = [&](u32 h2) {
#pragma unroll
    for (u32 e = 0; e < 4; ++e) dma16(srdW, vu, ((h2 * 4u + e + koff) & 7u) * 64u * wstr, SLOT(h2) + e * 8192u + w * 1024u);
  };
  const v4u srdW2 = mk_srd(p.w2, 1376u * 1024u);
  const v4u srdWo = mk_srd(p.wo, 512u * 1024u);
  const u32 vp = (4u * hh + rr) * 1024u + nb * 128u + g1 * 64u + 16u * q4;
  auto piece = [&](v4u srd, u32 row0, u32 dst, u32 n, u32 nw, u32 iw) {
#pragma unroll
    for (u32 e = 0; e < n; ++e) dma16(srd, vp, (row0 + (e * nw + iw) * 8u) * 1024u, dst + (e * nw + iw) * 1024u);
  };

  const u32 rot = r ^ (2u * hf);
  const u32 xa0 = (4u * r + (l31 >> 3)) * 1024u + (l31 & 7u) * 64u + ((16u * (hh ^ ((l31 >> 2) & 3u))) ^ ((rot & 1u) * 32u)) + (rot >> 1) * 512u;
  const u32 wl = hf * 256u + rr * 64u + g1 * 32u + q4 * 8u;
  const u32 wa0 = 16384u + rot * 4096u + hh * 2048u + wl;
  const u32 one2 = 0x3f803f80u;
  const u32 selv = ((lane & 15u) == (g1 ? 4u : 0u)) ? one2 : 0u;
  const v4u sel = {selv, selv, selv, selv};

  v4u xr[32];
  v4u wA[4], wB[4];
  f16v acc0, acc1;
  f4 cs0 = {0.f, 0.f, 0.f, 0.f}, cs1 = {0.f, 0.f, 0.f, 0.f};
#pragma unroll
  for (int i = 0; i < 16; ++i) { acc0[i] = 0.f; acc1[i] = 0.f; }
  float sum = 0.f, sq = 0.f;
  auto stats = [&](v4u v) {
#pragma unroll
    for (int i = 0; i < 4; ++i) {
      const float lo = blo(v[i]), hi = bhi(v[i]);
      sum += lo; sq = __builtin_fmaf(lo, lo, sq);
      sum += hi; sq = __builtin_fmaf(hi, hi, sq);
    }
    asm volatile("" : "+v"(sum), "+v"(sq));
  };
#pragma unroll
  for (u32 cp = 0; cp < 8; ++cp) {
    BAR_VM(8);
    if (cp == 0) STAMP(1);
    if (cp < 5) item_chunk(cp + 3);
    else if (cp < 7) item_u2(cp - 5);
    else {
      piece(srdWo, 0u, SLOT(2), 4, 8, w);
      const u32 c = (rb & 3u) * 8u + w;
      dma4(c < 22u ? srdW2 : srdWo, ((c < 22u ? c : c - 22u) * 64u + lane) * 1024u + nb * 128u, SM_DUMMY);
    }
    const u32 sl = SLOT(cp & 3u);
#pragma unroll
    for (u32 s = 0; s < 2; ++s) {
      xr[cp * 4 + s] = lds16(sl + (xa0 ^ (s * 32u)));
      wA[s * 2] = ldsfrag(sl + (wa0 ^ (s * 4096u)), 1024u);
      wA[s * 2 + 1] = ldsfrag(sl + (wa0 ^ (s * 4096u)) + 512u, 1024u);
    }
    FENCE();
    if (cp > 0) {
      acc0 = mfma32(wB[0], xr[cp * 4 - 2], acc0); acc1 = mfma32(wB[1], xr[cp * 4 - 2], acc1);
      acc0 = mfma32(wB[2], xr[cp * 4 - 1], acc0); acc1 = mfma32(wB[3], xr[cp * 4 - 1], acc1);
      stats(xr[cp * 4 - 3]);
    }
    FENCE();
#pragma unroll
    for (u32 s = 2; s < 4; ++s) {
      xr[cp * 4 + s] = lds16(sl + (xa0 ^ (512u + (s & 1u) * 32u)));
      wB[s * 2 - 4] = ldsfrag(sl + (wa0 ^ (s * 4096u)), 1024u);
      wB[s * 2 - 3] = ldsfrag(sl + (wa0 ^ (s * 4096u)) + 512u, 1024u);
    }
    FENCE();
    acc0 = mfma32(wA[0], xr[cp * 4], acc0); acc1 = mfma32(wA[1], xr[cp * 4], acc1);
    cs0 = mfma16(sel, wA[0], cs0); cs1 = mfma16(sel, wA[1], cs1);
    acc0 = mfma32(wA[2], xr[cp * 4 + 1], acc0); acc1 = mfma32(wA[3], xr[cp * 4 + 1], acc1);
    stats(xr[cp * 4]);
    FENCE();
  }
  acc0 = mfma32(wB[0], xr[30], acc0); acc1 = mfma32(wB[1], xr[30], acc1);
  acc0 = mfma32(wB[2], xr[31], acc0); acc1 = mfma32(wB[3], xr[31], acc1);
  stats(xr[29]);
  *(LAS f2*)(SC_STAT + (32u * r + l31) * 32u + (hf * 2u + hh) * 8u) = (f2){sum, sq};
  if (lane < 32u) {
    *(LAS float*)(SC_CS + (hf * 32u + lane) * 16u + r * 4u) = cs0[0];
    *(LAS float*)(SC_CS + 1024u + (hf * 32u + lane) * 16u + r * 4u) = cs1[0];
  }
  u32 tidv = tid;
  asm volatile("" : "+v"(tidv));
  auto tail = [&](auto ismc, auto hfc) {
  constexpr bool ISM = decltype(ismc)::v; constexpr u32 hf = decltype(hfc)::v;
  const u32 tid = tidv, lane = tid & 63u;
  const u32 hh = lane >> 5, l31 = lane & 31u, q4 = lane & 3u, rr = (lane >> 2) & 3u, g1 = (lane >> 4) & 1u;
  const u32 wl = hf * 256u + rr * 64u + g1 * 32u + q4 * 8u;
  const u32 selv = ((lane & 15u) == (g1 ? 4u : 0u)) ? one2 : 0u;
  const v4u sel = {selv, selv, selv, selv};
  const u32 vp = (4u * hh + rr) * 1024u + nb * 128u + g1 * 64u + 16u * q4;
  auto piece = [&](v4u srd, u32 row0, u32 dst, u32 n, u32 nw, u32 iw) {
#pragma unroll
    for (u32 e = 0; e < n; ++e) dma16(srd, vp, (row0 + (e * nw + iw) * 8u) * 1024u, dst + (e * nw + iw) * 1024u);
  };
  if constexpr (ISM) { BAR_VM(5); } else { BAR(); }
  STAMP(2);

  const u32 ep = __builtin_amdgcn_readfirstlane(*(LAS u32*)SM_E);
  const u32 tag = ep * 1664525u + 1013904223u, tag2 = (ep ^ 0x9e3779b9u) * 2654435761u + 12345u;
  const __amdgpu_buffer_rsrc_t rsW = __builtin_amdgcn_make_buffer_rsrc(p.ws, 0, WS_END, 0x00020000);
  auto put_flag = [&](u32 off) {
    __builtin_amdgcn_raw_buffer_store_b64((v2u){tag, tag2}, rsW, lane == 0u ? off : 0x80000000u, 0, 16);
  };

  float mu, var;
  {
    const f4 a = *(LAS f4*)(SC_STAT + (32u * r + l31) * 32u), c = *(LAS f4*)(SC_STAT + (32u * r + l31) * 32u + 16u);
    mu = ((a[0] + a[2]) + (c[0] + c[2])) * (1.f / 512.f);
    var = fmaxf(((a[1] + a[3]) + (c[1] + c[3])) * (1.f / 512.f) - mu * mu, 0.f);
  }
  const float sig2 = ISM ? __builtin_fmaf(1e-5f, var + 1e-5f, var) : var + 1e-5f;
  const float rinv = __builtin_amdgcn_rsqf(sig2);
  const float sig = sig2 * rinv;
  v4u fx;
  {
    const u32 mh = pk(-mu, -mu);
    const u32 sh = pk(-mu - blo(mh), sig);
    const u32 sl_ = pk(sig - bhi(sh), 0.f);
    fx = (v4u){hh ? 0u : mh, hh ? 0u : sh, hh ? 0u : sl_, 0u};
  }
  auto cs_total = [&](u32 a) { const f4 c = *(LAS f4*)(a + (hf * 32u + l31) * 16u); return (c[0] + c[1]) + (c[2] + c[3]); };
  const u32 ua0 = rot * 2048u + hh * 1024u + wl;
  f16v acc2;
  f4 cs2 = {0.f, 0.f, 0.f, 0.f};
  auto g2 = [&](int c0, int c1) {
#pragma unroll
    for (int cp = c0; cp < c1; ++cp) {
      v4u u[4];
#pragma unroll
      for (u32 s = 0; s < 4; ++s) u[s] = ldsfrag(cp * 8192u + (ua0 ^ (s * 2048u)), 512u);
      cs2 = mfma16(sel, u[0], cs2);
#pragma unroll
      for (u32 s = 0; s < 4; ++s) acc2 = mfma32(u[s], xr[cp * 4 + s], acc2);
    }
  };
#pragma unroll
  for (int i = 0; i < 16; ++i) acc2[i] = 0.f;

  const u32 mrow = w & 1u, kh = (w >> 1) & 1u;
  const u32 pa0 = hh * 512u + rr * 64u + g1 * 32u + q4 * 8u;
  const u32 tok32 = 4u * t + r;
  v4u wr[32];
  auto to_regs = [&]() {
    if constexpr (hf) {
#pragma unroll
      for (u32 i = 0; i < 16; ++i) {
        const u32 a = SLOT(2) + kh * 32768u + ((i + 2u * nb) & 15u) * 2048u + pa0;
        wr[2 * i] = ldsfrag(a, 1024u); wr[2 * i + 1] = ldsfrag(a + 256u, 1024u);
      }
    } else {
#pragma unroll
      for (u32 i = 0; i < 5; ++i) {
        const u32 a = SC_V + (kh * 5u + i) * 2048u + pa0;
        wr[2 * i] = ldsfrag(a, 1024u); wr[2 * i + 1] = ldsfrag(a + 256u, 1024u);
      }
    }
  };

  if constexpr (ISM) {
    const float cA = cs_total(SC_CS), cB = cs_total(SC_CS + 1024u);
    const u32 bA = *(LAS unsigned short*)(SM_WB + hf * 64u + l31 * 2u), bB = *(LAS unsigned short*)(SM_WB + 128u + hf * 64u + l31 * 2u);
    const u32 bC = *(LAS unsigned short*)(SM_WB + 256u + (six ? hf * 64u : 0u) + l31 * 2u);
    BAR();
    if constexpr (hf) { piece(srdWo, 256u, SLOT(3), 8, 4, r); piece(srdW2, 1216u, SC_V, 5, 4, r); }
    acc0 = mfma32(fold_w(cA, bA), fx, acc0);
    acc1 = mfma32(fold_w(cB, bB), fx, acc1);
    const float c0 = -2.302208198f * rinv;
    const float c1 = -0.1029432397f * rinv * rinv * rinv;
    const u32 hb = WS_H + (tok32 * 86u + seg0 * 2u) * 1024u + lane * 16u;
    if constexpr (hf) g2(0, 8);
    gelu16(acc0, c0, c1, sig);
    __builtin_amdgcn_raw_buffer_store_b128(pk8(acc0, 0), rsW, hb + hf * 2048u, 0, 16);
    __builtin_amdgcn_raw_buffer_store_b128(pk8(acc0, 8), rsW, hb + hf * 2048u + 1024u, 0, 16);
    gelu16(acc1, c0, c1, sig);
    __builtin_amdgcn_raw_buffer_store_b128(pk8(acc1, 0), rsW, hb + hf * 2048u + 4096u, 0, 16);
    __builtin_amdgcn_raw_buffer_store_b128(pk8(acc1, 8), rsW, hb + hf * 2048u + 5120u, 0, 16);
    if (!hf && six) g2(0, 8);
    if (lane < 32u) *(LAS float*)(SC_CS2 + (hf * 32u + lane) * 16u + r * 4u) = cs2[0];
    BAR_VM(4);
    STAMP(5);
    if constexpr (hf) piece(srdW2, 0u, SLOT(0), 16, 4, r);
    const float cC = cs_total(SC_CS2);
    to_regs();
    if (hf || six) {
      acc2 = mfma32(fold_w(cC, bC), fx, acc2);
      gelu16(acc2, c0, c1, sig);
      const u32 h2 = hb + 8192u + (six ? hf * 2048u : 0u);
      __builtin_amdgcn_raw_buffer_store_b128(pk8(acc2, 0), rsW, h2, 0, 16);
      __builtin_amdgcn_raw_buffer_store_b128(pk8(acc2, 8), rsW, h2 + 1024u, 0, 16);
    }
    BAR();
    STAMP(6);
    if constexpr (hf) { piece(srdW2, 512u, SLOT(2), 22, 4, r); WAIT_VM(22); } else { WAIT_VM(0); }
    put_flag(WS_FH + (tok32 * 16u + idx * 2u + hf) * FSTR);
    STAMP(9);
  } else {
    const float cA = cs_total(SC_CS), cB = cs_total(SC_CS + 1024u);
    v4u aphi[2];
    const u32 sgn = (lane & 4u) ? 0x80008000u : 0u;
#pragma unroll
    for (u32 e = 0; e < 2; ++e) {
      aphi[e] = ldsfrag(SM_WB + (2u * hf + e) * 512u + (4u * hh + rr) * 32u + (2u * g1 + (q4 >> 1)) * 8u, 256u);
      aphi[e] ^= (v4u){sgn, sgn, sgn, sgn};
    }
    acc0 = mfma32(fold_w(cA, 0u), fx, acc0);
    acc1 = mfma32(fold_w(cB, 0u), fx, acc1);
    const float cl = rinv * 1.4426950409f;
    f16v zero;
#pragma unroll
    for (int i = 0; i < 16; ++i) zero[i] = 0.f;
    const u32 tokl = 32u * r + l31, sw = (l31 >> 2) & 1u;
#pragma unroll
    for (u32 e = 0; e < 2; ++e) {
      const f16v pp = mfma32(aphi[e], pk8(acc0, 8 * e), zero);
      v4u o0, o1; softmax16(pp, cl, o0, o1);
      const u32 a = SLOT(3) + ((2u * hf + e) * 2u + hh) * 4096u + tokl * 32u;
      *(LAS v4u*)(a + sw * 16u) = o0; *(LAS v4u*)(a + (sw ^ 1u) * 16u) = o1;
    }
#pragma unroll
    for (int i = 0; i < 16; ++i) acc1[i] *= rinv;
#pragma unroll
    for (u32 e = 0; e < 2; ++e) *(LAS v4u*)(SC_V + (2u * hf + e) * 4096u + tokl * 32u + (hh ^ sw) * 16u) = pk8(acc1, 8 * e);
    BAR_VM(5);
    STAMP(3);
    {
      const u32 la = (16u * hh + 4u * g1 + rr) * 32u + ((q4 * 8u) ^ (g1 * 16u));
      f4 akv = {0.f, 0.f, 0.f, 0.f}, az = {0.f, 0.f, 0.f, 0.f};
      const v4u ones = {one2, one2, one2, one2};
#pragma unroll
      for (u32 ks = 0; ks < 4; ++ks) {
        const v4u fa = ldsfrag(SLOT(3) + (r * 2u + hf) * 4096u + ks * 1024u + la, 256u);
        const v4u fb = ldsfrag(SC_V + r * 4096u + ks * 1024u + la, 256u);
        akv = mfma16(fa, fb, akv); az = mfma16(fa, ones, az);
      }
      const u32 pi = (idx * 8u + w) * 16u + t;
      __builtin_amdgcn_raw_buffer_store_b64((v2u){pk(akv[0], akv[1]), pk(akv[2], akv[3])}, rsW, WS_KVP + pi * 512u + lane * 8u, 0, 16);
      __builtin_amdgcn_raw_buffer_store_b128(__builtin_bit_cast(v4u, az), rsW, (lane & 15u) == 0u ? WS_ZP + pi * 64u + (lane >> 4) * 16u : 0x80000000u, 0, 16);
      BAR();
      STAMP(4);
      if constexpr (hf) { piece(srdWo, 256u, SLOT(3), 8, 4, r); piece(srdW2, 1216u, SC_V, 5, 4, r); }
      g2(0, 4);
      if constexpr (hf) { WAIT_VM(13); } else { WAIT_VM(0); }
      put_flag(WS_FKV + pi * FSTR);
    }
    g2(4, 8);
    if (lane < 32u) *(LAS float*)(SC_CS2 + (hf * 32u + lane) * 16u + r * 4u) = cs2[0];
    BAR_VM(1);
    STAMP(5);
    if constexpr (hf) piece(srdW2, 0u, SLOT(0), 16, 4, r);
    const float cC = cs_total(SC_CS2);
    to_regs();
    acc2 = mfma32(fold_w(cC, 0u), fx, acc2);
    v4u phq[4];
#pragma unroll
    for (u32 e = 0; e < 2; ++e) {
      const f16v pp = mfma32(aphi[e], pk8(acc2, 8 * e), zero);
      softmax16(pp, cl, phq[2 * e], phq[2 * e + 1]);
    }
    STAMP(6);
    wait_flags(rsW, WS_FKV + ((idx * 8u + w) * 16u + (lane & 15u)) * FSTR, tag, tag2);
    STAMP(7);
    {
      v4u pv[8];
#pragma unroll
      for (u32 i = 0; i < 8; ++i) pv[i] = __builtin_amdgcn_raw_buffer_load_b128(rsW, WS_KVP + (idx * 8u + w) * 8192u + lane * 16u + i * 1024u, 0, 16);
      f4 z = __builtin_bit_cast(f4, __builtin_amdgcn_raw_buffer_load_b128(rsW, WS_ZP + (idx * 8u + w) * 1024u + lane * 16u, 0, 16));
      float s8[8];
#pragma unroll
      for (int j = 0; j < 4; ++j) { s8[2 * j] = blo(pv[0][j]); s8[2 * j + 1] = bhi(pv[0][j]); }
#pragma unroll
      for (int i = 1; i < 8; ++i)
#pragma unroll
        for (int j = 0; j < 4; ++j) { s8[2 * j] += blo(pv[i][j]); s8[2 * j + 1] += bhi(pv[i][j]); }
      float tt[4];
#pragma unroll
      for (int j = 0; j < 4; ++j) {
        const v2u sw2 = __builtin_amdgcn_permlane32_swap(__float_as_uint(s8[j]), __float_as_uint(s8[4 + j]), false, false);
        tt[j] = __uint_as_float(sw2.x) + __uint_as_float(sw2.y);
      }
      const u32 c = ((2u * l31) & 15u) + hh, d = (c & 3u) + 8u * ((c >> 2) & 1u) + 4u * (c >> 3);
      *(LAS v2u*)(SM_KVT + w * 544u + d * 32u + (l31 >> 3) * 8u) = (v2u){pk(tt[0], tt[1]), pk(tt[2], tt[3])};
#pragma unroll
      for (int j = 0; j < 4; ++j) {
        float v = z[j];
        v += __uint_as_float(__builtin_amdgcn_update_dpp(0u, __float_as_uint(v), 0x124, 0xf, 0xf, false));
        v += __uint_as_float(__builtin_amdgcn_update_dpp(0u, __float_as_uint(v), 0x128, 0xf, 0xf, false));
        v2u s2 = __builtin_amdgcn_permlane16_swap(__float_as_uint(v), __float_as_uint(v), false, false);
        v = __uint_as_float(s2.x) + __uint_as_float(s2.y);
        s2 = __builtin_amdgcn_permlane32_swap(__float_as_uint(v), __float_as_uint(v), false, false);
        z[j] = __uint_as_float(s2.x) + __uint_as_float(s2.y);
      }
      if (lane < 4u) *(LAS v2u*)(SM_KVT + w * 544u + 512u + lane * 8u) = (v2u){pk(z[0], z[1]), pk(z[2], z[3])};
    }
    BAR();
    STAMP(8);
#pragma unroll
    for (u32 e = 0; e < 2; ++e) {
      const u32 a = SM_KVT + (2u * hf + e + 4u * hh) * 544u + (l31 < 16u ? l31 : 16u) * 32u;
      f16v d = mfma32(lds16(a), phq[2 * e], zero);
      d = mfma32(lds16(a + 16u), phq[2 * e + 1], d);
      const float rd = __builtin_amdgcn_rcpf(d[8] + 1e-6f);
#pragma unroll
      for (int i = 0; i < 8; ++i) d[i] *= rd;
      __builtin_amdgcn_raw_buffer_store_b128(pk8(d, 0), rsW, WS_O + (tok32 * 32u + idx * 4u + 2u * hf + e) * 1024u + lane * 16u, 0, 16);
    }
    if constexpr (hf) { piece(srdW2, 512u, SLOT(2), 22, 4, r); WAIT_VM(22); } else { WAIT_VM(0); }
    put_flag(WS_FO + (tok32 * 16u + idx * 2u + hf) * FSTR);
    STAMP(9);
  }

  const u32 tk = 2u * tb + mrow;
  f16v d0, d1;
#pragma unroll
  for (int i = 0; i < 16; ++i) { d0[i] = 0.f; d1[i] = 0.f; }
  v4u xres;
  const u32 xo = (64u * tb + (tid >> 3)) * 1024u + nb * 128u + (tid & 7u) * 16u;
  if constexpr (!hf) {
    constexpr int WIN = 24;
    wait_flags(rsW, WS_FH + (tk * 16u + (lane & 15u)) * FSTR, tag, tag2);
    STAMP(10);
    const u32 hv = WS_H + tk * 86u * 1024u + lane * 16u;
    const u32 st0 = nb * 5u;
    auto kstep = [&](int i) -> u32 {
      if (i < 5) return 76u + kh * 5u + i;
      u32 k = st0 + (i - 5); k = k >= 38u ? k - 38u : k;
      return kh * 38u + k;
    };
    v4u act[43];
#pragma unroll
    for (int i = 0; i < WIN; ++i) act[i] = __builtin_amdgcn_raw_buffer_load_b128(rsW, hv, kstep(i) * 1024u, 16);
    xres = *(const v4u*)(p.x + xo);
    FENCE();
    BAR();
    STAMP(11);
    v4u n0, n1;
#pragma unroll
    for (int i = 0; i < 43; ++i) {
      v4u c0, c1;
      if (i < 5) { c0 = wr[2 * i]; c1 = wr[2 * i + 1]; } else { c0 = n0; c1 = n1; }
      if (i + 1 >= 5 && i + 1 < 43) { const u32 a = kstep(i + 1) * 2048u + pa0; n0 = ldsfrag(a, 1024u); n1 = ldsfrag(a + 256u, 1024u); }
      FENCE();
      d0 = mfma32(c0, act[i], d0); d1 = mfma32(c1, act[i], d1);
      if (i + WIN < 43) act[i + WIN] = __builtin_amdgcn_raw_buffer_load_b128(rsW, hv, kstep(i + WIN) * 1024u, 16);
      FENCE();
    }
  } else {
    BAR_VM(0);
    STAMP(11);
    xres = *(const v4u*)(p.x + xo);
    if (b == 0u && w == 7u) {
      wait_flags(rsW, WS_FH + ((lane >> 3) * 64u + (lane & 7u) * 2u) * FSTR, tag, tag2);
      wait_flags(rsW, WS_FH + ((lane >> 3) * 64u + 512u + (lane & 7u) * 2u) * FSTR, tag, tag2);
    }
    wait_flags(rsW, WS_FO + (tk * 16u + kh * 8u + (lane & 7u)) * FSTR, tag, tag2);
    STAMP(10);
    const u32 ov = WS_O + (tk * 32u + kh * 16u) * 1024u + lane * 16u;
    v4u act[16];
#pragma unroll
    for (u32 i = 0; i < 16; ++i) act[i] = __builtin_amdgcn_raw_buffer_load_b128(rsW, ov, ((i + 2u * nb) & 15u) * 1024u, 16);
    FENCE();
#pragma unroll
    for (u32 i = 0; i < 16; ++i) { d0 = mfma32(wr[2 * i], act[i], d0); d1 = mfma32(wr[2 * i + 1], act[i], d1); }
  }
  STAMP(12);
  BAR();
  if (b == 0u && w == 7u) __builtin_amdgcn_raw_buffer_store_b32(tag, rsW, lane == 0u ? WS_E : 0x80000000u, 0, 16);
  {
    const u32 a = (hf * 2u + kh) * 17408u + (32u * mrow + l31) * 272u + hh * 16u;
#pragma unroll
    for (int g = 0; g < 4; ++g) {
      *(LAS f4*)(a + g * 32u) = (f4){d0[4 * g], d0[4 * g + 1], d0[4 * g + 2], d0[4 * g + 3]};
      *(LAS f4*)(a + 128u + g * 32u) = (f4){d1[4 * g], d1[4 * g + 1], d1[4 * g + 2], d1[4 * g + 3]};
    }
  }
  BAR();
  STAMP(13);
  {
    const u32 a = (tid >> 3) * 272u + (tid & 7u) * 32u;
    const v4u bb = lds16(SM_B2 + nb * 128u + (tid & 7u) * 16u);
    float res[8];
#pragma unroll
    for (int hx = 0; hx < 2; ++hx) {
      const f4 m0 = *(LAS f4*)(a + hx * 16u), m1 = *(LAS f4*)(a + 17408u + hx * 16u);
      const f4 s0 = *(LAS f4*)(a + 34816u + hx * 16u), s1 = *(LAS f4*)(a + 52224u + hx * 16u);
#pragma unroll
      for (int j = 0; j < 4; ++j) {
        const u32 bw = bb[hx * 2 + (j >> 1)], xw = xres[hx * 2 + (j >> 1)];
        const float m = (m0[j] + m1[j]) + ((j & 1) ? bhi(bw) : blo(bw));
        const float g = m * __builtin_amdgcn_rcpf(1.f + __builtin_amdgcn_exp2f(m * -1.4426950409f));
        res[hx * 4 + j] = __builtin_fmaf(s0[j] + s1[j], g, (j & 1) ? bhi(xw) : blo(xw));
      }
    }
    *(v4u*)(p.out + xo) = (v4u){pk(res[0], res[1]), pk(res[2], res[3]), pk(res[4], res[5]), pk(res[6], res[7])};
  }
  STAMP(14);
  };
  auto finish = [&]() {
    asm volatile("s_endpgm" : "=v"(xr[0]), "=v"(xr[1]), "=v"(xr[2]), "=v"(xr[3]), "=v"(xr[4]), "=v"(xr[5]), "=v"(xr[6]), "=v"(xr[7]),
                 "=v"(xr[8]), "=v"(xr[9]), "=v"(xr[10]), "=v"(xr[11]), "=v"(xr[12]), "=v"(xr[13]), "=v"(xr[14]), "=v"(xr[15]), "=v"(acc0), "=v"(acc1) :: "memory");
    asm volatile("" : "=v"(xr[16]), "=v"(xr[17]), "=v"(xr[18]), "=v"(xr[19]), "=v"(xr[20]), "=v"(xr[21]), "=v"(xr[22]), "=v"(xr[23]),
                 "=v"(xr[24]), "=v"(xr[25]), "=v"(xr[26]), "=v"(xr[27]), "=v"(xr[28]), "=v"(xr[29]), "=v"(xr[30]), "=v"(xr[31]), "=v"(tidv) :: "memory");
  };
  u32 role = isM * 2u + hf;
  if (role == 0u) { tail(IC<0>{}, IC<0>{}); finish(); }
  asm volatile("" : "+s"(role));
  if (role == 1u) { tail(IC<0>{}, IC<1>{}); finish(); }
  asm volatile("" : "+s"(role));
  if (role == 2u) { tail(IC<1>{}, IC<0>{}); finish(); }
  tail(IC<1>{}, IC<1>{});
  asm volatile(".Lhh_end:");
}

extern "C" void kernel_launch(void* const* d_in, const int* in_sizes, int n_in,
                              void* d_out, int out_size, void* d_ws, size_t ws_size,
                              hipStream_t stream) {
  Params p;
  p.x = (const char*)d_in[0]; p.wqkv = (const char*)d_in[1]; p.wphi = (const char*)d_in[2]; p.wo = (const char*)d_in[3];
  p.w1 = (const char*)d_in[5]; p.b1 = (const char*)d_in[6]; p.w2 = (const char*)d_in[7]; p.b2 = (const char*)d_in[8];
  p.out = (char*)d_out; p.ws = (char*)d_ws;
  (void)hipFuncSetAttribute((const void*)hh_fused, hipFuncAttributeMaxDynamicSharedMemorySize, (int)LDS_BYTES);
  hh_fused<<<256, 512, LDS_BYTES, stream>>>(p);
}
```

```cpp
#define PADSTAGE 14
#define PADT 5000ull
#include <hip/hip_runtime.h>
#include <hip/hip_bf16.h>
#pragma clang diagnostic ignored "-Wint-to-pointer-cast"

typedef unsigned u32;
typedef unsigned long long u64;
typedef u32 v4u __attribute__((ext_vector_type(4)));
typedef u32 v2u __attribute__((ext_vector_type(2)));
typedef float f2 __attribute__((ext_vector_type(2)));
typedef float f4 __attribute__((ext_vector_type(4)));
typedef float f16v __attribute__((ext_vector_type(16)));
typedef __bf16 bf2 __attribute__((ext_vector_type(2)));
typedef __bf16 bf8 __attribute__((ext_vector_type(8)));
typedef short s4 __attribute__((ext_vector_type(4)));
#define LAS __attribute__((address_space(3)))
#define DEV __device__ __forceinline__

#define FSTR 128u
#define WS_E 0u
#define WS_FKV 4096u
#define WS_FH (WS_FKV + 1024u * FSTR)
#define WS_FO (WS_FH + 1024u * FSTR)
#define WS_ZP (WS_FO + 1024u * FSTR)
#define WS_KVP (WS_ZP + 65536u)
#define WS_O (WS_KVP + 524288u)
#define WS_H (WS_O + 2097152u)
#define WS_END (WS_H + 64u * 86u * 1024u)
#define WS_STAMP (16u << 20)
#define SLOT(i) ((u32)(i) * 32768u)
#define SC_V 131072u
#define SC_STAT 147456u
#define SC_CS 151552u
#define SC_CS2 153600u
#define SM_WB 155648u
#define SM_B2 157696u
#define SM_KVT 158720u
#define SM_E 163072u
#define SM_DUMMY 163328u
#define LDS_BYTES 163840u
#define SPIN_CAP 60000

#define STAMP(i) do { if ((i) == PADSTAGE) { for (int sp_ = 0; sp_ < 100000; ++sp_) { u64 t_; asm volatile("s_memrealtime %0\n\ts_waitcnt lgkmcnt(0)" : "=s"(t_) :: "memory"); if (t_ - rt0 >= PADT) break; } } } while (0)

template <int N> struct IC { static constexpr int v = N; };
struct Params {
  const char* x; const char* wqkv; const char* wphi; const char* wo; const char* w1;
  const char* b1; const char* w2; const char* b2; char* out; char* ws;
};

DEV v4u mk_srd(const void* p, u32 bytes) {
  const u64 a = (u64)p;
  return (v4u){(u32)a, (u32)(a >> 32) & 0xffffu, bytes, 0x00020000u};
}
DEV void dma16(v4u srd, u32 voff, u32 soff, u32 dst) {
  asm volatile("s_mov_b32 m0, %0\n\ts_nop 0\n\tbuffer_load_dwordx4 %1, %2, %3 offen lds" :: "s"(dst), "v"(voff), "s"(srd), "s"(soff) : "memory");
}
DEV void dma4(v4u srd, u32 voff, u32 dst) {
  asm volatile("s_mov_b32 m0, %0\n\ts_nop 0\n\tbuffer_load_dword %1, %2, 0 offen lds" :: "s"(dst), "v"(voff), "s"(srd) : "memory");
}
DEV void dma4_sc1(v4u srd, u32 voff, u32 dst) {
  asm volatile("s_mov_b32 m0, %0\n\ts_nop 0\n\tbuffer_load_dword %1, %2, 0 offen sc1 lds" :: "s"(dst), "v"(voff), "s"(srd) : "memory");
}
#define WAIT_VM(n) asm volatile("s_waitcnt vmcnt(" #n ")" ::: "memory")
#define BAR_VM(n) asm volatile("s_waitcnt vmcnt(" #n ") lgkmcnt(0)\n\ts_barrier" ::: "memory")
#define BAR() asm volatile("s_waitcnt lgkmcnt(0)\n\ts_barrier" ::: "memory")
#define FENCE() __builtin_amdgcn_sched_barrier(0)

DEV v4u lds16(u32 a) { return *(LAS v4u*)a; }
DEV v2u ldstr(u32 a) { return __builtin_bit_cast(v2u, __builtin_amdgcn_ds_read_tr16_b64_v4i16((LAS s4*)a)); }
DEV v4u ldsfrag(u32 a, u32 d) { const v2u p = ldstr(a), q = ldstr(a + d); return (v4u){p.x, p.y, q.x, q.y}; }
DEV f16v mfma32(v4u a, v4u b, f16v c) {
  return __builtin_amdgcn_mfma_f32_32x32x16_bf16(__builtin_bit_cast(bf8, a), __builtin_bit_cast(bf8, b), c, 0, 0, 0); }
DEV f4 mfma16(v4u a, v4u b, f4 c) { return __builtin_amdgcn_mfma_f32_16x16x32_bf16(__builtin_bit_cast(bf8, a), __builtin_bit_cast(bf8, b), c, 0, 0, 0); }
DEV u32 pk(float a, float b) { return __builtin_bit_cast(u32, __builtin_convertvector((f2){a, b}, bf2)); }
DEV float blo(u32 v) { return __uint_as_float(v << 16); }
DEV float bhi(u32 v) { return __uint_as_float(v & 0xffff0000u); }
DEV v4u pk8(const f16v& a, int o) { return (v4u){pk(a[o], a[o + 1]), pk(a[o + 2], a[o + 3]), pk(a[o + 4], a[o + 5]), pk(a[o + 6], a[o + 7])}; }

DEV v4u fold_w(float cs, u32 b16) {
  const u32 h = pk(cs, cs) & 0xffffu;
  const u32 l = pk(cs - blo(h), 0.f);
  return (v4u){h | (l << 16), h | (b16 << 16), b16, 0u};
}
DEV void softmax16(const f16v& p, float c, v4u& o0, v4u& o1) {
  float mx = fmaxf(fmaxf(p[0], p[1]), p[2]);
#pragma unroll
  for (int i = 3; i < 15; i += 2) mx = fmaxf(fmaxf(mx, p[i]), p[i + 1]);
  mx = fmaxf(mx, p[15]);
  const float nm = -mx * c;
  f16v e; float s = 0.f;
#pragma unroll
  for (int i = 0; i < 16; ++i) { e[i] = __builtin_amdgcn_exp2f(__builtin_fmaf(p[i], c, nm)); s += e[i]; }
  const float rs = __builtin_amdgcn_rcpf(s);
#pragma unroll
  for (int i = 0; i < 16; ++i) e[i] *= rs;
  o0 = pk8(e, 0); o1 = pk8(e, 8);
}
DEV void gelu16(f16v& a, float c0, float c1, float sig) {
#pragma unroll
  for (int i = 0; i < 16; ++i) {
    const float t = a[i] * a[i];
    const float e = __builtin_amdgcn_exp2f(a[i] * __builtin_fmaf(t, c1, c0));
    a[i] = a[i] * __builtin_amdgcn_rcpf(__builtin_fmaf(e, sig, sig));
  }
}
DEV void wait_flags(__amdgpu_buffer_rsrc_t rs, u32 voff, u32 tag, u32 tag2) {
#pragma unroll 1
  for (int sp = 0; sp < SPIN_CAP; ++sp) {
    const v2u f = __builtin_amdgcn_raw_buffer_load_b64(rs, voff, 0, 16);
    if (__all(f.x == tag && f.y == tag2)) break;
    asm volatile("" ::: "memory");
  }
}

__global__ void __launch_bounds__(512, 2) hh_fused(Params p) {
  const u32 tid = threadIdx.x, lane = tid & 63u;
  const u32 w = __builtin_amdgcn_readfirstlane(tid >> 6);
  const u32 b = blockIdx.x;
  u64 rt0; asm volatile("s_memrealtime %0\n\ts_waitcnt lgkmcnt(0)" : "=s"(rt0) :: "memory");
  const u32 xcd = b & 7u, rb = b >> 3, xg = xcd >> 1, gl = rb >> 3;
  const u32 isM = gl & 1u;
  const u32 idx = xg * 2u + (gl >> 1);
  const u32 t = (xcd & 1u) * 8u + (rb & 7u);
  const u32 koff = ((rb & 7u) + 2u * gl) & 7u;
  const u32 tb = (xcd & 1u) * 16u + xg * 4u + (rb & 3u), nb = rb >> 2;
  const u32 r = w & 3u, hf = w >> 2;
  const u32 hh = lane >> 5, l31 = lane & 31u, q4 = lane & 3u, rr = (lane >> 2) & 3u, g1 = (lane >> 4) & 1u;

  const v4u srdWs = mk_srd(p.ws, WS_END);
  const u32 seg0 = idx <= 3u ? 6u * idx : 18u + 5u * (idx - 3u);
  const u32 six = idx < 3u;
  if (w < 4u) {
    u64 pc; u32 len;
    asm volatile("s_getpc_b64 %0\n.Lhh_pc:\n\ts_mov_b32 %1, .Lhh_end-.Lhh_pc" : "=s"(pc), "=s"(len));
    dma4(mk_srd((const void*)pc, len), w * 8192u + lane * 128u, SM_DUMMY);
  } else if (w < 6u) {
    if (isM) dma16(mk_srd(p.b1, 2752u), seg0 * 64u + lane * 16u, 0u, SM_WB + (w - 4u) * 1024u);
    else dma16(mk_srd(p.wphi, 16384u), idx * 2048u + (w - 4u) * 1024u + lane * 16u, 0u, SM_WB + (w - 4u) * 1024u);
  } else if (w == 6u) {
    dma16(mk_srd(p.b2, 1024u), lane * 16u, 0u, SM_B2);
  } else {
    dma4_sc1(srdWs, lane * 131072u + (lane ? 2048u : 0u), SM_E);
  }

  const u32 wstr = isM ? 2752u : 3072u;
  const v4u srdX = mk_srd(p.x, 2097152u);
  const v4u srdW = mk_srd(isM ? p.w1 : p.wqkv, 512u * wstr);
  const u32 row7 = (lane >> 2) & 7u;
  const u32 vx0 = (16u * w + row7) * 1024u + hh * 64u + 16u * (q4 ^ (row7 >> 2));
  const u32 vx1 = (vx0 + 8192u) ^ 32u;
  const u32 colb = isM ? seg0 * 64u : 1024u + idx * 128u;
  const u32 vw = (8u * w + rr) * wstr + colb + g1 * 64u + hh * (isM ? 128u : 1024u) + 16u * q4;
  const u32 xs = t * 131072u;
  auto item_chunk = [&](u32 cp) {
    const u32 kc = (cp + koff) & 7u, sl = SLOT(cp & 3u);
    dma16(srdX, vx0, xs + kc * 128u, sl + w * 2048u);
    dma16(srdW, vw, kc * 64u * wstr, sl + 16384u + w * 2048u);
    dma16(srdX, vx1, xs + kc * 128u, sl + w * 2048u + 1024u);
    dma16(srdW, vw, (kc * 64u + 4u) * wstr, sl + 16384u + w * 2048u + 1024u);
  };
  item_chunk(0); item_chunk(1); item_chunk(2);

  const u32 vu = (8u * w + 4u * hh + rr) * wstr + (isM ? seg0 * 64u + 256u + (six ? g1 * 64u : 0u) : idx * 128u + g1 * 64u) + 16u * q4;
  auto item_u2 = [&](u32 h2) {
#pragma unroll
    for (u32 e = 0; e < 4; ++e) dma16(srdW, vu, ((h2 * 4u + e + koff) & 7u) * 64u * wstr, SLOT(h2) + e * 8192u + w * 1024u);
  };
  const v4u srdW2 = mk_srd(p.w2, 1376u * 1024u);
  const v4u srdWo = mk_srd(p.wo, 512u * 1024u);
  const u32 vp = (4u * hh + rr) * 1024u + nb * 128u + g1 * 64u + 16u * q4;
  auto piece = [&](v4u srd, u32 row0, u32 dst, u32 n, u32 nw, u32 iw) {
#pragma unroll
    for (u32 e = 0; e < n; ++e) dma16(srd, vp, (row0 + (e * nw + iw) * 8u) * 1024u, dst + (e * nw + iw) * 1024u);
  };

  const u32 rot = r ^ (2u * hf);
  const u32 xa0 = (4u * r + (l31 >> 3)) * 1024u + (l31 & 7u) * 64u + ((16u * (hh ^ ((l31 >> 2) & 3u))) ^ ((rot & 1u) * 32u)) + (rot >> 1) * 512u;
  const u32 wl = hf * 256u + rr * 64u + g1 * 32u + q4 * 8u;
  const u32 wa0 = 16384u + rot * 4096u + hh * 2048u + wl;
  const u32 one2 = 0x3f803f80u;
  const u32 selv = ((lane & 15u) == (g1 ? 4u : 0u)) ? one2 : 0u;
  const v4u sel = {selv, selv, selv, selv};

  v4u xr[32];
  v4u wA[4], wB[4];
  f16v acc0, acc1;
  f4 cs0 = {0.f, 0.f, 0.f, 0.f}, cs1 = {0.f, 0.f, 0.f, 0.f};
#pragma unroll
  for (int i = 0; i < 16; ++i) { acc0[i] = 0.f; acc1[i] = 0.f; }
  float sum = 0.f, sq = 0.f;
  auto stats = [&](v4u v) {
#pragma unroll
    for (int i = 0; i < 4; ++i) {
      const float lo = blo(v[i]), hi = bhi(v[i]);
      sum += lo; sq = __builtin_fmaf(lo, lo, sq);
      sum += hi; sq = __builtin_fmaf(hi, hi, sq);
    }
    asm volatile("" : "+v"(sum), "+v"(sq));
  };
#pragma unroll
  for (u32 cp = 0; cp < 8; ++cp) {
    BAR_VM(8);
    if (cp == 0) STAMP(1);
    if (cp < 5) item_chunk(cp + 3);
    else if (cp < 7) item_u2(cp - 5);
    else {
      piece(srdWo, 0u, SLOT(2), 4, 8, w);
      const u32 c = (rb & 3u) * 8u + w;
      dma4(c < 22u ? srdW2 : srdWo, ((c < 22u ? c : c - 22u) * 64u + lane) * 1024u + nb * 128u, SM_DUMMY);
    }
    const u32 sl = SLOT(cp & 3u);
#pragma unroll
    for (u32 s = 0; s < 2; ++s) {
      xr[cp * 4 + s] = lds16(sl + (xa0 ^ (s * 32u)));
      wA[s * 2] = ldsfrag(sl + (wa0 ^ (s * 4096u)), 1024u);
      wA[s * 2 + 1] = ldsfrag(sl + (wa0 ^ (s * 4096u)) + 512u, 1024u);
    }
    FENCE();
    if (cp > 0) {
      acc0 = mfma32(wB[0], xr[cp * 4 - 2], acc0); acc1 = mfma32(wB[1], xr[cp * 4 - 2], acc1);
      acc0 = mfma32(wB[2], xr[cp * 4 - 1], acc0); acc1 = mfma32(wB[3], xr[cp * 4 - 1], acc1);
      stats(xr[cp * 4 - 3]);
    }
    FENCE();
#pragma unroll
    for (u32 s = 2; s < 4; ++s) {
      xr[cp * 4 + s] = lds16(sl + (xa0 ^ (512u + (s & 1u) * 32u)));
      wB[s * 2 - 4] = ldsfrag(sl + (wa0 ^ (s * 4096u)), 1024u);
      wB[s * 2 - 3] = ldsfrag(sl + (wa0 ^ (s * 4096u)) + 512u, 1024u);
    }
    FENCE();
    acc0 = mfma32(wA[0], xr[cp * 4], acc0); acc1 = mfma32(wA[1], xr[cp * 4], acc1);
    cs0 = mfma16(sel, wA[0], cs0); cs1 = mfma16(sel, wA[1], cs1);
    acc0 = mfma32(wA[2], xr[cp * 4 + 1], acc0); acc1 = mfma32(wA[3], xr[cp * 4 + 1], acc1);
    stats(xr[cp * 4]);
    FENCE();
  }
  acc0 = mfma32(wB[0], xr[30], acc0); acc1 = mfma32(wB[1], xr[30], acc1);
  acc0 = mfma32(wB[2], xr[31], acc0); acc1 = mfma32(wB[3], xr[31], acc1);
  stats(xr[29]);
  *(LAS f2*)(SC_STAT + (32u * r + l31) * 32u + (hf * 2u + hh) * 8u) = (f2){sum, sq};
  if (lane < 32u) {
    *(LAS float*)(SC_CS + (hf * 32u + lane) * 16u + r * 4u) = cs0[0];
    *(LAS float*)(SC_CS + 1024u + (hf * 32u + lane) * 16u + r * 4u) = cs1[0];
  }
  u32 tidv = tid;
  asm volatile("" : "+v"(tidv));
  auto tail = [&](auto ismc, auto hfc) {
  constexpr bool ISM = decltype(ismc)::v; constexpr u32 hf = decltype(hfc)::v;
  const u32 tid = tidv, lane = tid & 63u;
  const u32 hh = lane >> 5, l31 = lane & 31u, q4 = lane & 3u, rr = (lane >> 2) & 3u, g1 = (lane >> 4) & 1u;
  const u32 wl = hf * 256u + rr * 64u + g1 * 32u + q4 * 8u;
  const u32 selv = ((lane & 15u) == (g1 ? 4u : 0u)) ? one2 : 0u;
  const v4u sel = {selv, selv, selv, selv};
  const u32 vp = (4u * hh + rr) * 1024u + nb * 128u + g1 * 64u + 16u * q4;
  auto piece = [&](v4u srd, u32 row0, u32 dst, u32 n, u32 nw, u32 iw) {
#pragma unroll
    for (u32 e = 0; e < n; ++e) dma16(srd, vp, (row0 + (e * nw + iw) * 8u) * 1024u, dst + (e * nw + iw) * 1024u);
  };
  if constexpr (ISM) { BAR_VM(5); } else { BAR(); }
  STAMP(2);

  const u32 ep = __builtin_amdgcn_readfirstlane(*(LAS u32*)SM_E);
  const u32 tag = ep * 1664525u + 1013904223u, tag2 = (ep ^ 0x9e3779b9u) * 2654435761u + 12345u;
  const __amdgpu_buffer_rsrc_t rsW = __builtin_amdgcn_make_buffer_rsrc(p.ws, 0, WS_END, 0x00020000);
  auto put_flag = [&](u32 off) {
    __builtin_amdgcn_raw_buffer_store_b64((v2u){tag, tag2}, rsW, lane == 0u ? off : 0x80000000u, 0, 16);
  };

  float mu, var;
  {
    const f4 a = *(LAS f4*)(SC_STAT + (32u * r + l31) * 32u), c = *(LAS f4*)(SC_STAT + (32u * r + l31) * 32u + 16u);
    mu = ((a[0] + a[2]) + (c[0] + c[2])) * (1.f / 512.f);
    var = fmaxf(((a[1] + a[3]) + (c[1] + c[3])) * (1.f / 512.f) - mu * mu, 0.f);
  }
  const float sig2 = ISM ? __builtin_fmaf(1e-5f, var + 1e-5f, var) : var + 1e-5f;
  const float rinv = __builtin_amdgcn_rsqf(sig2);
  const float sig = sig2 * rinv;
  v4u fx;
  {
    const u32 mh = pk(-mu, -mu);
    const u32 sh = pk(-mu - blo(mh), sig);
    const u32 sl_ = pk(sig - bhi(sh), 0.f);
    fx = (v4u){hh ? 0u : mh, hh ? 0u : sh, hh ? 0u : sl_, 0u};
  }
  auto cs_total = [&](u32 a) { const f4 c = *(LAS f4*)(a + (hf * 32u + l31) * 16u); return (c[0] + c[1]) + (c[2] + c[3]); };
  const u32 ua0 = rot * 2048u + hh * 1024u + wl;
  f16v acc2;
  f4 cs2 = {0.f, 0.f, 0.f, 0.f};
  auto g2 = [&](int c0, int c1) {
#pragma unroll
    for (int cp = c0; cp < c1; ++cp) {
      v4u u[4];
#pragma unroll
      for (u32 s = 0; s < 4; ++s) u[s] = ldsfrag(cp * 8192u + (ua0 ^ (s * 2048u)), 512u);
      cs2 = mfma16(sel, u[0], cs2);
#pragma unroll
      for (u32 s = 0; s < 4; ++s) acc2 = mfma32(u[s], xr[cp * 4 + s], acc2);
    }
  };
#pragma unroll
  for (int i = 0; i < 16; ++i) acc2[i] = 0.f;

  const u32 mrow = w & 1u, kh = (w >> 1) & 1u;
  const u32 pa0 = hh * 512u + rr * 64u + g1 * 32u + q4 * 8u;
  const u32 tok32 = 4u * t + r;
  v4u wr[32];
  auto to_regs = [&]() {
    if constexpr (hf) {
#pragma unroll
      for (u32 i = 0; i < 16; ++i) {
        const u32 a = SLOT(2) + kh * 32768u + ((i + 2u * nb) & 15u) * 2048u + pa0;
        wr[2 * i] = ldsfrag(a, 1024u); wr[2 * i + 1] = ldsfrag(a + 256u, 1024u);
      }
    } else {
#pragma unroll
      for (u32 i = 0; i < 5; ++i) {
        const u32 a = SC_V + (kh * 5u + i) * 2048u + pa0;
        wr[2 * i] = ldsfrag(a, 1024u); wr[2 * i + 1] = ldsfrag(a + 256u, 1024u);
      }
    }
  };

  if constexpr (ISM) {
    const float cA = cs_total(SC_CS), cB = cs_total(SC_CS + 1024u);
    const u32 bA = *(LAS unsigned short*)(SM_WB + hf * 64u + l31 * 2u), bB = *(LAS unsigned short*)(SM_WB + 128u + hf * 64u + l31 * 2u);
    const u32 bC = *(LAS unsigned short*)(SM_WB + 256u + (six ? hf * 64u : 0u) + l31 * 2u);
    BAR();
    if constexpr (hf) { piece(srdWo, 256u, SLOT(3), 8, 4, r); piece(srdW2, 1216u, SC_V, 5, 4, r); }
    acc0 = mfma32(fold_w(cA, bA), fx, acc0);
    acc1 = mfma32(fold_w(cB, bB), fx, acc1);
    const float c0 = -2.302208198f * rinv;
    const float c1 = -0.1029432397f * rinv * rinv * rinv;
    const u32 hb = WS_H + (tok32 * 86u + seg0 * 2u) * 1024u + lane * 16u;
    if constexpr (hf) g2(0, 8);
    gelu16(acc0, c0, c1, sig);
    __builtin_amdgcn_raw_buffer_store_b128(pk8(acc0, 0), rsW, hb + hf * 2048u, 0, 16);
    __builtin_amdgcn_raw_buffer_store_b128(pk8(acc0, 8), rsW, hb + hf * 2048u + 1024u, 0, 16);
    gelu16(acc1, c0, c1, sig);
    __builtin_amdgcn_raw_buffer_store_b128(pk8(acc1, 0), rsW, hb + hf * 2048u + 4096u, 0, 16);
    __builtin_amdgcn_raw_buffer_store_b128(pk8(acc1, 8), rsW, hb + hf * 2048u + 5120u, 0, 16);
    if (!hf && six) g2(0, 8);
    if (lane < 32u) *(LAS float*)(SC_CS2 + (hf * 32u + lane) * 16u + r * 4u) = cs2[0];
    BAR_VM(4);
    STAMP(5);
    if constexpr (hf) piece(srdW2, 0u, SLOT(0), 16, 4, r);
    const float cC = cs_total(SC_CS2);
    to_regs();
    if (hf || six) {
      acc2 = mfma32(fold_w(cC, bC), fx, acc2);
      gelu16(acc2, c0, c1, sig);
      const u32 h2 = hb + 8192u + (six ? hf * 2048u : 0u);
      __builtin_amdgcn_raw_buffer_store_b128(pk8(acc2, 0), rsW, h2, 0, 16);
      __builtin_amdgcn_raw_buffer_store_b128(pk8(acc2, 8), rsW, h2 + 1024u, 0, 16);
    }
    BAR();
    STAMP(6);
    if constexpr (hf) { piece(srdW2, 512u, SLOT(2), 22, 4, r); WAIT_VM(22); } else { WAIT_VM(0); }
    put_flag(WS_FH + (tok32 * 16u + idx * 2u + hf) * FSTR);
    STAMP(9);
  } else {
    const float cA = cs_total(SC_CS), cB = cs_total(SC_CS + 1024u);
    v4u aphi[2];
    const u32 sgn = (lane & 4u) ? 0x80008000u : 0u;
#pragma unroll
    for (u32 e = 0; e < 2; ++e) {
      aphi[e] = ldsfrag(SM_WB + (2u * hf + e) * 512u + (4u * hh + rr) * 32u + (2u * g1 + (q4 >> 1)) * 8u, 256u);
      aphi[e] ^= (v4u){sgn, sgn, sgn, sgn};
    }
    acc0 = mfma32(fold_w(cA, 0u), fx, acc0);
    acc1 = mfma32(fold_w(cB, 0u), fx, acc1);
    const float cl = rinv * 1.4426950409f;
    f16v zero;
#pragma unroll
    for (int i = 0; i < 16; ++i) zero[i] = 0.f;
    const u32 tokl = 32u * r + l31, sw = (l31 >> 2) & 1u;
#pragma unroll
    for (u32 e = 0; e < 2; ++e) {
      const f16v pp = mfma32(aphi[e], pk8(acc0, 8 * e), zero);
      v4u o0, o1; softmax16(pp, cl, o0, o1);
      const u32 a = SLOT(3) + ((2u * hf + e) * 2u + hh) * 4096u + tokl * 32u;
      *(LAS v4u*)(a + sw * 16u) = o0; *(LAS v4u*)(a + (sw ^ 1u) * 16u) = o1;
    }
#pragma unroll
    for (int i = 0; i < 16; ++i) acc1[i] *= rinv;
#pragma unroll
    for (u32 e = 0; e < 2; ++e) *(LAS v4u*)(SC_V + (2u * hf + e) * 4096u + tokl * 32u + (hh ^ sw) * 16u) = pk8(acc1, 8 * e);
    BAR_VM(5);
    STAMP(3);
    {
      const u32 la = (16u * hh + 4u * g1 + rr) * 32u + ((q4 * 8u) ^ (g1 * 16u));
      f4 akv = {0.f, 0.f, 0.f, 0.f}, az = {0.f, 0.f, 0.f, 0.f};
      const v4u ones = {one2, one2, one2, one2};
#pragma unroll
      for (u32 ks = 0; ks < 4; ++ks) {
        const v4u fa = ldsfrag(SLOT(3) + (r * 2u + hf) * 4096u + ks * 1024u + la, 256u);
        const v4u fb = ldsfrag(SC_V + r * 4096u + ks * 1024u + la, 256u);
        akv = mfma16(fa, fb, akv); az = mfma16(fa, ones, az);
      }
      const u32 pi = (idx * 8u + w) * 16u + t;
      __builtin_amdgcn_raw_buffer_store_b64((v2u){pk(akv[0], akv[1]), pk(akv[2], akv[3])}, rsW, WS_KVP + pi * 512u + lane * 8u, 0, 16);
      __builtin_amdgcn_raw_buffer_store_b128(__builtin_bit_cast(v4u, az), rsW, (lane & 15u) == 0u ? WS_ZP + pi * 64u + (lane >> 4) * 16u : 0x80000000u, 0, 16);
      BAR();
      STAMP(4);
      if constexpr (hf) { piece(srdWo, 256u, SLOT(3), 8, 4, r); piece(srdW2, 1216u, SC_V, 5, 4, r); }
      g2(0, 4);
      if constexpr (hf) { WAIT_VM(13); } else { WAIT_VM(0); }
      put_flag(WS_FKV + pi * FSTR);
    }
    g2(4, 8);
    if (lane < 32u) *(LAS float*)(SC_CS2 + (hf * 32u + lane) * 16u + r * 4u) = cs2[0];
    BAR_VM(1);
    STAMP(5);
    if constexpr (hf) piece(srdW2, 0u, SLOT(0), 16, 4, r);
    const float cC = cs_total(SC_CS2);
    to_regs();
    acc2 = mfma32(fold_w(cC, 0u), fx, acc2);
    v4u phq[4];
#pragma unroll
    for (u32 e = 0; e < 2; ++e) {
      const f16v pp = mfma32(aphi[e], pk8(acc2, 8 * e), zero);
      softmax16(pp, cl, phq[2 * e], phq[2 * e + 1]);
    }
    STAMP(6);
    wait_flags(rsW, WS_FKV + ((idx * 8u + w) * 16u + (lane & 15u)) * FSTR, tag, tag2);
    STAMP(7);
    {
      v4u pv[8];
#pragma unroll
      for (u32 i = 0; i < 8; ++i) pv[i] = __builtin_amdgcn_raw_buffer_load_b128(rsW, WS_KVP + (idx * 8u + w) * 8192u + lane * 16u + i * 1024u, 0, 16);
      f4 z = __builtin_bit_cast(f4, __builtin_amdgcn_raw_buffer_load_b128(rsW, WS_ZP + (idx * 8u + w) * 1024u + lane * 16u, 0, 16));
      float s8[8];
#pragma unroll
      for (int j = 0; j < 4; ++j) { s8[2 * j] = blo(pv[0][j]); s8[2 * j + 1] = bhi(pv[0][j]); }
#pragma unroll
      for (int i = 1; i < 8; ++i)
#pragma unroll
        for (int j = 0; j < 4; ++j) { s8[2 * j] += blo(pv[i][j]); s8[2 * j + 1] += bhi(pv[i][j]); }
      float tt[4];
#pragma unroll
      for (int j = 0; j < 4; ++j) {
        const v2u sw2 = __builtin_amdgcn_permlane32_swap(__float_as_uint(s8[j]), __float_as_uint(s8[4 + j]), false, false);
        tt[j] = __uint_as_float(sw2.x) + __uint_as_float(sw2.y);
      }
      const u32 c = ((2u * l31) & 15u) + hh, d = (c & 3u) + 8u * ((c >> 2) & 1u) + 4u * (c >> 3);
      *(LAS v2u*)(SM_KVT + w * 544u + d * 32u + (l31 >> 3) * 8u) = (v2u){pk(tt[0], tt[1]), pk(tt[2], tt[3])};
#pragma unroll
      for (int j = 0; j < 4; ++j) {
        float v = z[j];
        v += __uint_as_float(__builtin_amdgcn_update_dpp(0u, __float_as_uint(v), 0x124, 0xf, 0xf, false));
        v += __uint_as_float(__builtin_amdgcn_update_dpp(0u, __float_as_uint(v), 0x128, 0xf, 0xf, false));
        v2u s2 = __builtin_amdgcn_permlane16_swap(__float_as_uint(v), __float_as_uint(v), false, false);
        v = __uint_as_float(s2.x) + __uint_as_float(s2.y);
        s2 = __builtin_amdgcn_permlane32_swap(__float_as_uint(v), __float_as_uint(v), false, false);
        z[j] = __uint_as_float(s2.x) + __uint_as_float(s2.y);
      }
      if (lane < 4u) *(LAS v2u*)(SM_KVT + w * 544u + 512u + lane * 8u) = (v2u){pk(z[0], z[1]), pk(z[2], z[3])};
    }
    BAR();
    STAMP(8);
#pragma unroll
    for (u32 e = 0; e < 2; ++e) {
      const u32 a = SM_KVT + (2u * hf + e + 4u * hh) * 544u + (l31 < 16u ? l31 : 16u) * 32u;
      f16v d = mfma32(lds16(a), phq[2 * e], zero);
      d = mfma32(lds16(a + 16u), phq[2 * e + 1], d);
      const float rd = __builtin_amdgcn_rcpf(d[8] + 1e-6f);
#pragma unroll
      for (int i = 0; i < 8; ++i) d[i] *= rd;
      __builtin_amdgcn_raw_buffer_store_b128(pk8(d, 0), rsW, WS_O + (tok32 * 32u + idx * 4u + 2u * hf + e) * 1024u + lane * 16u, 0, 16);
    }
    if constexpr (hf) { piece(srdW2, 512u, SLOT(2), 22, 4, r); WAIT_VM(22); } else { WAIT_VM(0); }
    put_flag(WS_FO + (tok32 * 16u + idx * 2u + hf) * FSTR);
    STAMP(9);
  }

  const u32 tk = 2u * tb + mrow;
  f16v d0, d1;
#pragma unroll
  for (int i = 0; i < 16; ++i) { d0[i] = 0.f; d1[i] = 0.f; }
  v4u xres;
  const u32 xo = (64u * tb + (tid >> 3)) * 1024u + nb * 128u + (tid & 7u) * 16u;
  if constexpr (!hf) {
    constexpr int WIN = 24;
    wait_flags(rsW, WS_FH + (tk * 16u + (lane & 15u)) * FSTR, tag, tag2);
    STAMP(10);
    const u32 hv = WS_H + tk * 86u * 1024u + lane * 16u;
    const u32 st0 = nb * 5u;
    auto kstep = [&](int i) -> u32 {
      if (i < 5) return 76u + kh * 5u + i;
      u32 k = st0 + (i - 5); k = k >= 38u ? k - 38u : k;
      return kh * 38u + k;
    };
    v4u act[43];
#pragma unroll
    for (int i = 0; i < WIN; ++i) act[i] = __builtin_amdgcn_raw_buffer_load_b128(rsW, hv, kstep(i) * 1024u, 16);
    xres = *(const v4u*)(p.x + xo);
    FENCE();
    BAR();
    STAMP(11);
    v4u n0, n1;
#pragma unroll
    for (int i = 0; i < 43; ++i) {
      v4u c0, c1;
      if (i < 5) { c0 = wr[2 * i]; c1 = wr[2 * i + 1]; } else { c0 = n0; c1 = n1; }
      if (i + 1 >= 5 && i + 1 < 43) { const u32 a = kstep(i + 1) * 2048u + pa0; n0 = ldsfrag(a, 1024u); n1 = ldsfrag(a + 256u, 1024u); }
      FENCE();
      d0 = mfma32(c0, act[i], d0); d1 = mfma32(c1, act[i], d1);
      if (i + WIN < 43) act[i + WIN] = __builtin_amdgcn_raw_buffer_load_b128(rsW, hv, kstep(i + WIN) * 1024u, 16);
      FENCE();
    }
  } else {
    BAR_VM(0);
    STAMP(11);
    xres = *(const v4u*)(p.x + xo);
    if (b == 0u && w == 7u) {
      wait_flags(rsW, WS_FH + ((lane >> 3) * 64u + (lane & 7u) * 2u) * FSTR, tag, tag2);
      wait_flags(rsW, WS_FH + ((lane >> 3) * 64u + 512u + (lane & 7u) * 2u) * FSTR, tag, tag2);
    }
    wait_flags(rsW, WS_FO + (tk * 16u + kh * 8u + (lane & 7u)) * FSTR, tag, tag2);
    STAMP(10);
    const u32 ov = WS_O + (tk * 32u + kh * 16u) * 1024u + lane * 16u;
    v4u act[16];
#pragma unroll
    for (u32 i = 0; i < 16; ++i) act[i] = __builtin_amdgcn_raw_buffer_load_b128(rsW, ov, ((i + 2u * nb) & 15u) * 1024u, 16);
    FENCE();
#pragma unroll
    for (u32 i = 0; i < 16; ++i) { d0 = mfma32(wr[2 * i], act[i], d0); d1 = mfma32(wr[2 * i + 1], act[i], d1); }
  }
  STAMP(12);
  BAR();
  if (b == 0u && w == 7u) __builtin_amdgcn_raw_buffer_store_b32(tag, rsW, lane == 0u ? WS_E : 0x80000000u, 0, 16);
  {
    const u32 a = (hf * 2u + kh) * 17408u + (32u * mrow + l31) * 272u + hh * 16u;
#pragma unroll
    for (int g = 0; g < 4; ++g) {
      *(LAS f4*)(a + g * 32u) = (f4){d0[4 * g], d0[4 * g + 1], d0[4 * g + 2], d0[4 * g + 3]};
      *(LAS f4*)(a + 128u + g * 32u) = (f4){d1[4 * g], d1[4 * g + 1], d1[4 * g + 2], d1[4 * g + 3]};
    }
  }
  BAR();
  STAMP(13);
  {
    const u32 a = (tid >> 3) * 272u + (tid & 7u) * 32u;
    const v4u bb = lds16(SM_B2 + nb * 128u + (tid & 7u) * 16u);
    float res[8];
#pragma unroll
    for (int hx = 0; hx < 2; ++hx) {
      const f4 m0 = *(LAS f4*)(a + hx * 16u), m1 = *(LAS f4*)(a + 17408u + hx * 16u);
      const f4 s0 = *(LAS f4*)(a + 34816u + hx * 16u), s1 = *(LAS f4*)(a + 52224u + hx * 16u);
#pragma unroll
      for (int j = 0; j < 4; ++j) {
        const u32 bw = bb[hx * 2 + (j >> 1)], xw = xres[hx * 2 + (j >> 1)];
        const float m = (m0[j] + m1[j]) + ((j & 1) ? bhi(bw) : blo(bw));
        const float g = m * __builtin_amdgcn_rcpf(1.f + __builtin_amdgcn_exp2f(m * -1.4426950409f));
        res[hx * 4 + j] = __builtin_fmaf(s0[j] + s1[j], g, (j & 1) ? bhi(xw) : blo(xw));
      }
    }
    *(v4u*)(p.out + xo) = (v4u){pk(res[0], res[1]), pk(res[2], res[3]), pk(res[4], res[5]), pk(res[6], res[7])};
  }
  STAMP(14);
  };
  auto finish = [&]() {
    asm volatile("s_endpgm" : "=v"(xr[0]), "=v"(xr[1]), "=v"(xr[2]), "=v"(xr[3]), "=v"(xr[4]), "=v"(xr[5]), "=v"(xr[6]), "=v"(xr[7]),
                 "=v"(xr[8]), "=v"(xr[9]), "=v"(xr[10]), "=v"(xr[11]), "=v"(xr[12]), "=v"(xr[13]), "=v"(xr[14]), "=v"(xr[15]), "=v"(acc0), "=v"(acc1) :: "memory");
    asm volatile("" : "=v"(xr[16]), "=v"(xr[17]), "=v"(xr[18]), "=v"(xr[19]), "=v"(xr[20]), "=v"(xr[21]), "=v"(xr[22]), "=v"(xr[23]),
                 "=v"(xr[24]), "=v"(xr[25]), "=v"(xr[26]), "=v"(xr[27]), "=v"(xr[28]), "=v"(xr[29]), "=v"(xr[30]), "=v"(xr[31]), "=v"(tidv) :: "memory");
  };
  u32 role = isM * 2u + hf;
  if (role == 0u) { tail(IC<0>{}, IC<0>{}); finish(); }
  asm volatile("" : "+s"(role));
  if (role == 1u) { tail(IC<0>{}, IC<1>{}); finish(); }
  asm volatile("" : "+s"(role));
  if (role == 2u) { tail(IC<1>{}, IC<0>{}); finish(); }
  tail(IC<1>{}, IC<1>{});
  asm volatile(".Lhh_end:");
}

__global__ void hh_null(char* p) { if (p == nullptr) __builtin_trap(); }
extern "C" void kernel_launch(void* const* d_in, const int* in_sizes, int n_in,
                              void* d_out, int out_size, void* d_ws, size_t ws_size,
                              hipStream_t stream) {
  Params p;
  p.x = (const char*)d_in[0]; p.wqkv = (const char*)d_in[1]; p.wphi = (const char*)d_in[2]; p.wo = (const char*)d_in[3];
  p.w1 = (const char*)d_in[5]; p.b1 = (const char*)d_in[6]; p.w2 = (const char*)d_in[7]; p.b2 = (const char*)d_in[8];
  p.out = (char*)d_out; p.ws = (char*)d_ws;
  (void)hipFuncSetAttribute((const void*)hh_fused, hipFuncAttributeMaxDynamicSharedMemorySize, (int)LDS_BYTES);
  hh_fused<<<256, 512, LDS_BYTES, stream>>>(p);
  hh_null<<<32, 64, 0, stream>>>(p.ws);
}
```

```cpp
#define PADSTAGE 14
#define PADT 5000ull
#include <hip/hip_runtime.h>
#include <hip/hip_bf16.h>
#pragma clang diagnostic ignored "-Wint-to-pointer-cast"

typedef unsigned u32;
typedef unsigned long long u64;
typedef u32 v4u __attribute__((ext_vector_type(4)));
typedef u32 v2u __attribute__((ext_vector_type(2)));
typedef float f2 __attribute__((ext_vector_type(2)));
typedef float f4 __attribute__((ext_vector_type(4)));
typedef float f16v __attribute__((ext_vector_type(16)));
typedef __bf16 bf2 __attribute__((ext_vector_type(2)));
typedef __bf16 bf8 __attribute__((ext_vector_type(8)));
typedef short s4 __attribute__((ext_vector_type(4)));
#define LAS __attribute__((address_space(3)))
#define DEV __device__ __forceinline__

#define FSTR 128u
#define WS_E 0u
#define WS_FKV 4096u
#define WS_FH (WS_FKV + 1024u * FSTR)
#define WS_FO (WS_FH + 1024u * FSTR)
#define WS_ZP (WS_FO + 1024u * FSTR)
#define WS_KVP (WS_ZP + 65536u)
#define WS_O (WS_KVP + 524288u)
#define WS_H (WS_O + 2097152u)
#define WS_END (WS_H + 64u * 86u * 1024u)
#define WS_STAMP (16u << 20)
#define SLOT(i) ((u32)(i) * 32768u)
#define SC_V 131072u
#define SC_STAT 147456u
#define SC_CS 151552u
#define SC_CS2 153600u
#define SM_WB 155648u
#define SM_B2 157696u
#define SM_KVT 158720u
#define SM_E 163072u
#define SM_DUMMY 163328u
#define LDS_BYTES 163840u
#define SPIN_CAP 60000

#define STAMP(i) do { if ((i) == PADSTAGE) { for (int sp_ = 0; sp_ < 100000; ++sp_) { u64 t_; asm volatile("s_memrealtime %0\n\ts_waitcnt lgkmcnt(0)" : "=s"(t_) :: "memory"); if (t_ - rt0 >= PADT) break; } } } while (0)

template <int N> struct IC { static constexpr int v = N; };
struct Params {
  const char* x; const char* wqkv; const char* wphi; const char* wo; const char* w1;
  const char* b1; const char* w2; const char* b2; char* out; char* ws;
};

DEV v4u mk_srd(const void* p, u32 bytes) {
  const u64 a = (u64)p;
  return (v4u){(u32)a, (u32)(a >> 32) & 0xffffu, bytes, 0x00020000u};
}
DEV void dma16(v4u srd, u32 voff, u32 soff, u32 dst) {
  asm volatile("s_mov_b32 m0, %0\n\ts_nop 0\n\tbuffer_load_dwordx4 %1, %2, %3 offen lds" :: "s"(dst), "v"(voff), "s"(srd), "s"(soff) : "memory");
}
DEV void dma4(v4u srd, u32 voff, u32 dst) {
  asm volatile("s_mov_b32 m0, %0\n\ts_nop 0\n\tbuffer_load_dword %1, %2, 0 offen lds" :: "s"(dst), "v"(voff), "s"(srd) : "memory");
}
DEV void dma4_sc1(v4u srd, u32 voff, u32 dst) {
  asm volatile("s_mov_b32 m0, %0\n\ts_nop 0\n\tbuffer_load_dword %1, %2, 0 offen sc1 lds" :: "s"(dst), "v"(voff), "s"(srd) : "memory");
}
#define WAIT_VM(n) asm volatile("s_waitcnt vmcnt(" #n ")" ::: "memory")
#define BAR_VM(n) asm volatile("s_waitcnt vmcnt(" #n ") lgkmcnt(0)\n\ts_barrier" ::: "memory")
#define BAR() asm volatile("s_waitcnt lgkmcnt(0)\n\ts_barrier" ::: "memory")
#define FENCE() __builtin_amdgcn_sched_barrier(0)

DEV v4u lds16(u32 a) { return *(LAS v4u*)a; }
DEV v2u ldstr(u32 a) { return __builtin_bit_cast(v2u, __builtin_amdgcn_ds_read_tr16_b64_v4i16((LAS s4*)a)); }
DEV v4u ldsfrag(u32 a, u32 d) { const v2u p = ldstr(a), q = ldstr(a + d); return (v4u){p.x, p.y, q.x, q.y}; }
DEV f16v mfma32(v4u a, v4u b, f16v c) {
  return __builtin_amdgcn_mfma_f32_32x32x16_bf16(__builtin_bit_cast(bf8, a), __builtin_bit_cast(bf8, b), c, 0, 0, 0); }
DEV f4 mfma16(v4u a, v4u b, f4 c) { return __builtin_amdgcn_mfma_f32_16x16x32_bf16(__builtin_bit_cast(bf8, a), __builtin_bit_cast(bf8, b), c, 0, 0, 0); }
DEV u32 pk(float a, float b) { return __builtin_bit_cast(u32, __builtin_convertvector((f2){a, b}, bf2)); }
DEV float blo(u32 v) { return __uint_as_float(v << 16); }
DEV float bhi(u32 v) { return __uint_as_float(v & 0xffff0000u); }
DEV v4u pk8(const f16v& a, int o) { return (v4u){pk(a[o], a[o + 1]), pk(a[o + 2], a[o + 3]), pk(a[o + 4], a[o + 5]), pk(a[o + 6], a[o + 7])}; }

DEV v4u fold_w(float cs, u32 b16) {
  const u32 h = pk(cs, cs) & 0xffffu;
  const u32 l = pk(cs - blo(h), 0.f);
  return (v4u){h | (l << 16), h | (b16 << 16), b16, 0u};
}
DEV void softmax16(const f16v& p, float c, v4u& o0, v4u& o1) {
  float mx = fmaxf(fmaxf(p[0], p[1]), p[2]);
#pragma unroll
  for (int i = 3; i < 15; i += 2) mx = fmaxf(fmaxf(mx, p[i]), p[i + 1]);
  mx = fmaxf(mx, p[15]);
  const float nm = -mx * c;
  f16v e; float s = 0.f;
#pragma unroll
  for (int i = 0; i < 16; ++i) { e[i] = __builtin_amdgcn_exp2f(__builtin_fmaf(p[i], c, nm)); s += e[i]; }
  const float rs = __builtin_amdgcn_rcpf(s);
#pragma unroll
  for (int i = 0; i < 16; ++i) e[i] *= rs;
  o0 = pk8(e, 0); o1 = pk8(e, 8);
}
DEV void gelu16(f16v& a, float c0, float c1, float sig) {
#pragma unroll
  for (int i = 0; i < 16; ++i) {
    const float t = a[i] * a[i];
    const float e = __builtin_amdgcn_exp2f(a[i] * __builtin_fmaf(t, c1, c0));
    a[i] = a[i] * __builtin_amdgcn_rcpf(__builtin_fmaf(e, sig, sig));
  }
}
DEV void wait_flags(__amdgpu_buffer_rsrc_t rs, u32 voff, u32 tag, u32 tag2) {
#pragma unroll 1
  for (int sp = 0; sp < SPIN_CAP; ++sp) {
    const v2u f = __builtin_amdgcn_raw_buffer_load_b64(rs, voff, 0, 16);
    if (__all(f.x == tag && f.y == tag2)) break;
    asm volatile("" ::: "memory");
  }
}

__global__ void __launch_bounds__(512, 2) hh_fused(Params p) {
  const u32 tid = threadIdx.x, lane = tid & 63u;
  const u32 w = __builtin_amdgcn_readfirstlane(tid >> 6);
  const u32 b = blockIdx.x;
  u64 rt0; asm volatile("s_memrealtime %0\n\ts_waitcnt lgkmcnt(0)" : "=s"(rt0) :: "memory");
  const u32 xcd = b & 7u, rb = b >> 3, xg = xcd >> 1, gl = rb >> 3;
  const u32 isM = gl & 1u;
  const u32 idx = xg * 2u + (gl >> 1);
  const u32 t = (xcd & 1u) * 8u + (rb & 7u);
  const u32 koff = ((rb & 7u) + 2u * gl) & 7u;
  const u32 tb = (xcd & 1u) * 16u + xg * 4u + (rb & 3u), nb = rb >> 2;
  const u32 r = w & 3u, hf = w >> 2;
  const u32 hh = lane >> 5, l31 = lane & 31u, q4 = lane & 3u, rr = (lane >> 2) & 3u, g1 = (lane >> 4) & 1u;

  const v4u srdWs = mk_srd(p.ws, WS_END);
  const u32 seg0 = idx <= 3u ? 6u * idx : 18u + 5u * (idx - 3u);
  const u32 six = idx < 3u;
  if (w < 4u) {
    u64 pc; u32 len;
    asm volatile("s_getpc_b64 %0\n.Lhh_pc:\n\ts_mov_b32 %1, .Lhh_end-.Lhh_pc" : "=s"(pc), "=s"(len));
    dma4(mk_srd((const void*)pc, len), w * 8192u + lane * 128u, SM_DUMMY);
  } else if (w < 6u) {
    if (isM) dma16(mk_srd(p.b1, 2752u), seg0 * 64u + lane * 16u, 0u, SM_WB + (w - 4u) * 1024u);
    else dma16(mk_srd(p.wphi, 16384u), idx * 2048u + (w - 4u) * 1024u + lane * 16u, 0u, SM_WB + (w - 4u) * 1024u);
  } else if (w == 6u) {
    dma16(mk_srd(p.b2, 1024u), lane * 16u, 0u, SM_B2);
  } else {
    dma4_sc1(srdWs, lane * 131072u + (lane ? 2048u : 0u), SM_E);
  }

  const u32 wstr = isM ? 2752u : 3072u;
  const v4u srdX = mk_srd(p.x, 2097152u);
  const v4u srdW = mk_srd(isM ? p.w1 : p.wqkv, 512u * wstr);
  const u32 row7 = (lane >> 2) & 7u;
  const u32 vx0 = (16u * w + row7) * 1024u + hh * 64u + 16u * (q4 ^ (row7 >> 2));
  const u32 vx1 = (vx0 + 8192u) ^ 32u;
  const u32 colb = isM ? seg0 * 64u : 1024u + idx * 128u;
  const u32 vw = (8u * w + rr) * wstr + colb + g1 * 64u + hh * (isM ? 128u : 1024u) + 16u * q4;
  const u32 xs = t * 131072u;
  auto item_chunk = [&](u32 cp) {
    const u32 kc = (cp + koff) & 7u, sl = SLOT(cp & 3u);
    dma16(srdX, vx0, xs + kc * 128u, sl + w * 2048u);
    dma16(srdW, vw, kc * 64u * wstr, sl + 16384u + w * 2048u);
    dma16(srdX, vx1, xs + kc * 128u, sl + w * 2048u + 1024u);
    dma16(srdW, vw, (kc * 64u + 4u) * wstr, sl + 16384u + w * 2048u + 1024u);
  };
  item_chunk(0); item_chunk(1); item_chunk(2);

  const u32 vu = (8u * w + 4u * hh + rr) * wstr + (isM ? seg0 * 64u + 256u + (six ? g1 * 64u : 0u) : idx * 128u + g1 * 64u) + 16u * q4;
  auto item_u2 = [&](u32 h2) {
#pragma unroll
    for (u32 e = 0; e < 4; ++e) dma16(srdW, vu, ((h2 * 4u + e + koff) & 7u) * 64u * wstr, SLOT(h2) + e * 8192u + w * 1024u);
  };
  const v4u srdW2 = mk_srd(p.w2, 1376u * 1024u);
  const v4u srdWo = mk_srd(p.wo, 512u * 1024u);
  const u32 vp = (4u * hh + rr) * 1024u + nb * 128u + g1 * 64u + 16u * q4;
  auto piece = [&](v4u srd, u32 row0, u32 dst, u32 n, u32 nw, u32 iw) {
#pragma unroll
    for (u32 e = 0; e < n; ++e) dma16(srd, vp, (row0 + (e * nw + iw) * 8u) * 1024u, dst + (e * nw + iw) * 1024u);
  };

  const u32 rot = r ^ (2u * hf);
  const u32 xa0 = (4u * r + (l31 >> 3)) * 1024u + (l31 & 7u) * 64u + ((16u * (hh ^ ((l31 >> 2) & 3u))) ^ ((rot & 1u) * 32u)) + (rot >> 1) * 512u;
  const u32 wl = hf * 256u + rr * 64u + g1 * 32u + q4 * 8u;
  const u32 wa0 = 16384u + rot * 4096u + hh * 2048u + wl;
  const u32 one2 = 0x3f803f80u;
  const u32 selv = ((lane & 15u) == (g1 ? 4u : 0u)) ? one2 : 0u;
  const v4u sel = {selv, selv, selv, selv};

  v4u xr[32];
  v4u wA[4], wB[4];
  f16v acc0, acc1;
  f4 cs0 = {0.f, 0.f, 0.f, 0.f}, cs1 = {0.f, 0.f, 0.f, 0.f};
#pragma unroll
  for (int i = 0; i < 16; ++i) { acc0[i] = 0.f; acc1[i] = 0.f; }
  float sum = 0.f, sq = 0.f;
  auto stats = [&](v4u v) {
#pragma unroll
    for (int i = 0; i < 4; ++i) {
      const float lo = blo(v[i]), hi = bhi(v[i]);
      sum += lo; sq = __builtin_fmaf(lo, lo, sq);
      sum += hi; sq = __builtin_fmaf(hi, hi, sq);
    }
    asm volatile("" : "+v"(sum), "+v"(sq));
  };
#pragma unroll
  for (u32 cp = 0; cp < 8; ++cp) {
    BAR_VM(8);
    if (cp == 0) STAMP(1);
    if (cp < 5) item_chunk(cp + 3);
    else if (cp < 7) item_u2(cp - 5);
    else {
      piece(srdWo, 0u, SLOT(2), 4, 8, w);
      const u32 c = (rb & 3u) * 8u + w;
      dma4(c < 22u ? srdW2 : srdWo, ((c < 22u ? c : c - 22u) * 64u + lane) * 1024u + nb * 128u, SM_DUMMY);
    }
    const u32 sl = SLOT(cp & 3u);
#pragma unroll
    for (u32 s = 0; s < 2; ++s) {
      xr[cp * 4 + s] = lds16(sl + (xa0 ^ (s * 32u)));
      wA[s * 2] = ldsfrag(sl + (wa0 ^ (s * 4096u)), 1024u);
      wA[s * 2 + 1] = ldsfrag(sl + (wa0 ^ (s * 4096u)) + 512u, 1024u);
    }
    FENCE();
    if (cp > 0) {
      acc0 = mfma32(wB[0], xr[cp * 4 - 2], acc0); acc1 = mfma32(wB[1], xr[cp * 4 - 2], acc1);
      acc0 = mfma32(wB[2], xr[cp * 4 - 1], acc0); acc1 = mfma32(wB[3], xr[cp * 4 - 1], acc1);
      stats(xr[cp * 4 - 3]);
    }
    FENCE();
#pragma unroll
    for (u32 s = 2; s < 4; ++s) {
      xr[cp * 4 + s] = lds16(sl + (xa0 ^ (512u + (s & 1u) * 32u)));
      wB[s * 2 - 4] = ldsfrag(sl + (wa0 ^ (s * 4096u)), 1024u);
      wB[s * 2 - 3] = ldsfrag(sl + (wa0 ^ (s * 4096u)) + 512u, 1024u);
    }
    FENCE();
    acc0 = mfma32(wA[0], xr[cp * 4], acc0); acc1 = mfma32(wA[1], xr[cp * 4], acc1);
    cs0 = mfma16(sel, wA[0], cs0); cs1 = mfma16(sel, wA[1], cs1);
    acc0 = mfma32(wA[2], xr[cp * 4 + 1], acc0); acc1 = mfma32(wA[3], xr[cp * 4 + 1], acc1);
    stats(xr[cp * 4]);
    FENCE();
  }
  acc0 = mfma32(wB[0], xr[30], acc0); acc1 = mfma32(wB[1], xr[30], acc1);
  acc0 = mfma32(wB[2], xr[31], acc0); acc1 = mfma32(wB[3], xr[31], acc1);
  stats(xr[29]);
  *(LAS f2*)(SC_STAT + (32u * r + l31) * 32u + (hf * 2u + hh) * 8u) = (f2){sum, sq};
  if (lane < 32u) {
    *(LAS float*)(SC_CS + (hf * 32u + lane) * 16u + r * 4u) = cs0[0];
    *(LAS float*)(SC_CS + 1024u + (hf * 32u + lane) * 16u + r * 4u) = cs1[0];
  }
  u32 tidv = tid;
  asm volatile("" : "+v"(tidv));
  auto tail = [&](auto ismc, auto hfc) {
  constexpr bool ISM = decltype(ismc)::v; constexpr u32 hf = decltype(hfc)::v;
  const u32 tid = tidv, lane = tid & 63u;
  const u32 hh = lane >> 5, l31 = lane & 31u, q4 = lane & 3u, rr = (lane >> 2) & 3u, g1 = (lane >> 4) & 1u;
  const u32 wl = hf * 256u + rr * 64u + g1 * 32u + q4 * 8u;
  const u32 selv = ((lane & 15u) == (g1 ? 4u : 0u)) ? one2 : 0u;
  const v4u sel = {selv, selv, selv, selv};
  const u32 vp = (4u * hh + rr) * 1024u + nb * 128u + g1 * 64u + 16u * q4;
  auto piece = [&](v4u srd, u32 row0, u32 dst, u32 n, u32 nw, u32 iw) {
#pragma unroll
    for (u32 e = 0; e < n; ++e) dma16(srd, vp, (row0 + (e * nw + iw) * 8u) * 1024u, dst + (e * nw + iw) * 1024u);
  };
  if constexpr (ISM) { BAR_VM(5); } else { BAR(); }
  STAMP(2);

  const u32 ep = __builtin_amdgcn_readfirstlane(*(LAS u32*)SM_E);
  const u32 tag = ep * 1664525u + 1013904223u, tag2 = (ep ^ 0x9e3779b9u) * 2654435761u + 12345u;
  const __amdgpu_buffer_rsrc_t rsW = __builtin_amdgcn_make_buffer_rsrc(p.ws, 0, WS_END, 0x00020000);
  auto put_flag = [&](u32 off) {
    __builtin_amdgcn_raw_buffer_store_b64((v2u){tag, tag2}, rsW, lane == 0u ? off : 0x80000000u, 0, 16);
  };

  float mu, var;
  {
    const f4 a = *(LAS f4*)(SC_STAT + (32u * r + l31) * 32u), c = *(LAS f4*)(SC_STAT + (32u * r + l31) * 32u + 16u);
    mu = ((a[0] + a[2]) + (c[0] + c[2])) * (1.f / 512.f);
    var = fmaxf(((a[1] + a[3]) + (c[1] + c[3])) * (1.f / 512.f) - mu * mu, 0.f);
  }
  const float sig2 = ISM ? __builtin_fmaf(1e-5f, var + 1e-5f, var) : var + 1e-5f;
  const float rinv = __builtin_amdgcn_rsqf(sig2);
  const float sig = sig2 * rinv;
  v4u fx;
  {
    const u32 mh = pk(-mu, -mu);
    const u32 sh = pk(-mu - blo(mh), sig);
    const u32 sl_ = pk(sig - bhi(sh), 0.f);
    fx = (v4u){hh ? 0u : mh, hh ? 0u : sh, hh ? 0u : sl_, 0u};
  }
  auto cs_total = [&](u32 a) { const f4 c = *(LAS f4*)(a + (hf * 32u + l31) * 16u); return (c[0] + c[1]) + (c[2] + c[3]); };
  const u32 ua0 = rot * 2048u + hh * 1024u + wl;
  f16v acc2;
  f4 cs2 = {0.f, 0.f, 0.f, 0.f};
  auto g2 = [&](int c0, int c1) {
#pragma unroll
    for (int cp = c0; cp < c1; ++cp) {
      v4u u[4];
#pragma unroll
      for (u32 s = 0; s < 4; ++s) u[s] = ldsfrag(cp * 8192u + (ua0 ^ (s * 2048u)), 512u);
      cs2 = mfma16(sel, u[0], cs2);
#pragma unroll
      for (u32 s = 0; s < 4; ++s) acc2 = mfma32(u[s], xr[cp * 4 + s], acc2);
    }
  };
#pragma unroll
  for (int i = 0; i < 16; ++i) acc2[i] = 0.f;

  const u32 mrow = w & 1u, kh = (w >> 1) & 1u;
  const u32 pa0 = hh * 512u + rr * 64u + g1 * 32u + q4 * 8u;
  const u32 tok32 = 4u * t + r;
  v4u wr[32];
  auto to_regs = [&]() {
    if constexpr (hf) {
#pragma unroll
      for (u32 i = 0; i < 16; ++i) {
        const u32 a = SLOT(2) + kh * 32768u + ((i + 2u * nb) & 15u) * 2048u + pa0;
        wr[2 * i] = ldsfrag(a, 1024u); wr[2 * i + 1] = ldsfrag(a + 256u, 1024u);
      }
    } else {
#pragma unroll
      for (u32 i = 0; i < 5; ++i) {
        const u32 a = SC_V + (kh * 5u + i) * 2048u + pa0;
        wr[2 * i] = ldsfrag(a, 1024u); wr[2 * i + 1] = ldsfrag(a + 256u, 1024u);
      }
    }
  };

  if constexpr (ISM) {
    const float cA = cs_total(SC_CS), cB = cs_total(SC_CS + 1024u);
    const u32 bA = *(LAS unsigned short*)(SM_WB + hf * 64u + l31 * 2u), bB = *(LAS unsigned short*)(SM_WB + 128u + hf * 64u + l31 * 2u);
    const u32 bC = *(LAS unsigned short*)(SM_WB + 256u + (six ? hf * 64u : 0u) + l31 * 2u);
    BAR();
    if constexpr (hf) { piece(srdWo, 256u, SLOT(3), 8, 4, r); piece(srdW2, 1216u, SC_V, 5, 4, r); }
    acc0 = mfma32(fold_w(cA, bA), fx, acc0);
    acc1 = mfma32(fold_w(cB, bB), fx, acc1);
    const float c0 = -2.302208198f * rinv;
    const float c1 = -0.1029432397f * rinv * rinv * rinv;
    const u32 hb = WS_H + (tok32 * 86u + seg0 * 2u) * 1024u + lane * 16u;
    if constexpr (hf) g2(0, 8);
    gelu16(acc0, c0, c1, sig);
    __builtin_amdgcn_raw_buffer_store_b128(pk8(acc0, 0), rsW, hb + hf * 2048u, 0, 16);
    __builtin_amdgcn_raw_buffer_store_b128(pk8(acc0, 8), rsW, hb + hf * 2048u + 1024u, 0, 16);
    gelu16(acc1, c0, c1, sig);
    __builtin_amdgcn_raw_buffer_store_b128(pk8(acc1, 0), rsW, hb + hf * 2048u + 4096u, 0, 16);
    __builtin_amdgcn_raw_buffer_store_b128(pk8(acc1, 8), rsW, hb + hf * 2048u + 5120u, 0, 16);
    if (!hf && six) g2(0, 8);
    if (lane < 32u) *(LAS float*)(SC_CS2 + (hf * 32u + lane) * 16u + r * 4u) = cs2[0];
    BAR_VM(4);
    STAMP(5);
    if constexpr (hf) piece(srdW2, 0u, SLOT(0), 16, 4, r);
    const float cC = cs_total(SC_CS2);
    to_regs();
    if (hf || six) {
      acc2 = mfma32(fold_w(cC, bC), fx, acc2);
      gelu16(acc2, c0, c1, sig);
      const u32 h2 = hb + 8192u + (six ? hf * 2048u : 0u);
      __builtin_amdgcn_raw_buffer_store_b128(pk8(acc2, 0), rsW, h2, 0, 16);
      __builtin_amdgcn_raw_buffer_store_b128(pk8(acc2, 8), rsW, h2 + 1024u, 0, 16);
    }
    BAR();
    STAMP(6);
    if constexpr (hf) { piece(srdW2, 512u, SLOT(2), 22, 4, r); WAIT_VM(22); } else { WAIT_VM(0); }
    put_flag(WS_FH + (tok32 * 16u + idx * 2u + hf) * FSTR);
    STAMP(9);
  } else {
    const float cA = cs_total(SC_CS), cB = cs_total(SC_CS + 1024u);
    v4u aphi[2];
    const u32 sgn = (lane & 4u) ? 0x80008000u : 0u;
#pragma unroll
    for (u32 e = 0; e < 2; ++e) {
      aphi[e] = ldsfrag(SM_WB + (2u * hf + e) * 512u + (4u * hh + rr) * 32u + (2u * g1 + (q4 >> 1)) * 8u, 256u);
      aphi[e] ^= (v4u){sgn, sgn, sgn, sgn};
    }
    acc0 = mfma32(fold_w(cA, 0u), fx, acc0);
    acc1 = mfma32(fold_w(cB, 0u), fx, acc1);
    const float cl = rinv * 1.4426950409f;
    f16v zero;
#pragma unroll
    for (int i = 0; i < 16; ++i) zero[i] = 0.f;
    const u32 tokl = 32u * r + l31, sw = (l31 >> 2) & 1u;
#pragma unroll
    for (u32 e = 0; e < 2; ++e) {
      const f16v pp = mfma32(aphi[e], pk8(acc0, 8 * e), zero);
      v4u o0, o1; softmax16(pp, cl, o0, o1);
      const u32 a = SLOT(3) + ((2u * hf + e) * 2u + hh) * 4096u + tokl * 32u;
      *(LAS v4u*)(a + sw * 16u) = o0; *(LAS v4u*)(a + (sw ^ 1u) * 16u) = o1;
    }
#pragma unroll
    for (int i = 0; i < 16; ++i) acc1[i] *= rinv;
#pragma unroll
    for (u32 e = 0; e < 2; ++e) *(LAS v4u*)(SC_V + (2u * hf + e) * 4096u + tokl * 32u + (hh ^ sw) * 16u) = pk8(acc1, 8 * e);
    BAR_VM(5);
    STAMP(3);
    {
      const u32 la = (16u * hh + 4u * g1 + rr) * 32u + ((q4 * 8u) ^ (g1 * 16u));
      f4 akv = {0.f, 0.f, 0.f, 0.f}, az = {0.f, 0.f, 0.f, 0.f};
      const v4u ones = {one2, one2, one2, one2};
#pragma unroll
      for (u32 ks = 0; ks < 4; ++ks) {
        const v4u fa = ldsfrag(SLOT(3) + (r * 2u + hf) * 4096u + ks * 1024u + la, 256u);
        const v4u fb = ldsfrag(SC_V + r * 4096u + ks * 1024u + la, 256u);
        akv = mfma16(fa, fb, akv); az = mfma16(fa, ones, az);
      }
      const u32 pi = (idx * 8u + w) * 16u + t;
      __builtin_amdgcn_raw_buffer_store_b64((v2u){pk(akv[0], akv[1]), pk(akv[2], akv[3])}, rsW, WS_KVP + pi * 512u + lane * 8u, 0, 16);
      __builtin_amdgcn_raw_buffer_store_b128(__builtin_bit_cast(v4u, az), rsW, (lane & 15u) == 0u ? WS_ZP + pi * 64u + (lane >> 4) * 16u : 0x80000000u, 0, 16);
      BAR();
      STAMP(4);
      if constexpr (hf) { piece(srdWo, 256u, SLOT(3), 8, 4, r); piece(srdW2, 1216u, SC_V, 5, 4, r); }
      g2(0, 4);
      if constexpr (hf) { WAIT_VM(13); } else { WAIT_VM(0); }
      put_flag(WS_FKV + pi * FSTR);
    }
    g2(4, 8);
    if (lane < 32u) *(LAS float*)(SC_CS2 + (hf * 32u + lane) * 16u + r * 4u) = cs2[0];
    BAR_VM(1);
    STAMP(5);
    if constexpr (hf) piece(srdW2, 0u, SLOT(0), 16, 4, r);
    const float cC = cs_total(SC_CS2);
    to_regs();
    acc2 = mfma32(fold_w(cC, 0u), fx, acc2);
    v4u phq[4];
#pragma unroll
    for (u32 e = 0; e < 2; ++e) {
      const f16v pp = mfma32(aphi[e], pk8(acc2, 8 * e), zero);
      softmax16(pp, cl, phq[2 * e], phq[2 * e + 1]);
    }
    STAMP(6);
    wait_flags(rsW, WS_FKV + ((idx * 8u + w) * 16u + (lane & 15u)) * FSTR, tag, tag2);
    STAMP(7);
    {
      v4u pv[8];
#pragma unroll
      for (u32 i = 0; i < 8; ++i) pv[i] = __builtin_amdgcn_raw_buffer_load_b128(rsW, WS_KVP + (idx * 8u + w) * 8192u + lane * 16u + i * 1024u, 0, 16);
      f4 z = __builtin_bit_cast(f4, __builtin_amdgcn_raw_buffer_load_b128(rsW, WS_ZP + (idx * 8u + w) * 1024u + lane * 16u, 0, 16));
      float s8[8];
#pragma unroll
      for (int j = 0; j < 4; ++j) { s8[2 * j] = blo(pv[0][j]); s8[2 * j + 1] = bhi(pv[0][j]); }
#pragma unroll
      for (int i = 1; i < 8; ++i)
#pragma unroll
        for (int j = 0; j < 4; ++j) { s8[2 * j] += blo(pv[i][j]); s8[2 * j + 1] += bhi(pv[i][j]); }
      float tt[4];
#pragma unroll
      for (int j = 0; j < 4; ++j) {
        const v2u sw2 = __builtin_amdgcn_permlane32_swap(__float_as_uint(s8[j]), __float_as_uint(s8[4 + j]), false, false);
        tt[j] = __uint_as_float(sw2.x) + __uint_as_float(sw2.y);
      }
      const u32 c = ((2u * l31) & 15u) + hh, d = (c & 3u) + 8u * ((c >> 2) & 1u) + 4u * (c >> 3);
      *(LAS v2u*)(SM_KVT + w * 544u + d * 32u + (l31 >> 3) * 8u) = (v2u){pk(tt[0], tt[1]), pk(tt[2], tt[3])};
#pragma unroll
      for (int j = 0; j < 4; ++j) {
        float v = z[j];
        v += __uint_as_float(__builtin_amdgcn_update_dpp(0u, __float_as_uint(v), 0x124, 0xf, 0xf, false));
        v += __uint_as_float(__builtin_amdgcn_update_dpp(0u, __float_as_uint(v), 0x128, 0xf, 0xf, false));
        v2u s2 = __builtin_amdgcn_permlane16_swap(__float_as_uint(v), __float_as_uint(v), false, false);
        v = __uint_as_float(s2.x) + __uint_as_float(s2.y);
        s2 = __builtin_amdgcn_permlane32_swap(__float_as_uint(v), __float_as_uint(v), false, false);
        z[j] = __uint_as_float(s2.x) + __uint_as_float(s2.y);
      }
      if (lane < 4u) *(LAS v2u*)(SM_KVT + w * 544u + 512u + lane * 8u) = (v2u){pk(z[0], z[1]), pk(z[2], z[3])};
    }
    BAR();
    STAMP(8);
#pragma unroll
    for (u32 e = 0; e < 2; ++e) {
      const u32 a = SM_KVT + (2u * hf + e + 4u * hh) * 544u + (l31 < 16u ? l31 : 16u) * 32u;
      f16v d = mfma32(lds16(a), phq[2 * e], zero);
      d = mfma32(lds16(a + 16u), phq[2 * e + 1], d);
      const float rd = __builtin_amdgcn_rcpf(d[8] + 1e-6f);
#pragma unroll
      for (int i = 0; i < 8; ++i) d[i] *= rd;
      __builtin_amdgcn_raw_buffer_store_b128(pk8(d, 0), rsW, WS_O + (tok32 * 32u + idx * 4u + 2u * hf + e) * 1024u + lane * 16u, 0, 16);
    }
    if constexpr (hf) { piece(srdW2, 512u, SLOT(2), 22, 4, r); WAIT_VM(22); } else { WAIT_VM(0); }
    put_flag(WS_FO + (tok32 * 16u + idx * 2u + hf) * FSTR);
    STAMP(9);
  }

  const u32 tk = 2u * tb + mrow;
  f16v d0, d1;
#pragma unroll
  for (int i = 0; i < 16; ++i) { d0[i] = 0.f; d1[i] = 0.f; }
  v4u xres;
  const u32 xo = (64u * tb + (tid >> 3)) * 1024u + nb * 128u + (tid & 7u) * 16u;
  if constexpr (!hf) {
    constexpr int WIN = 24;
    wait_flags(rsW, WS_FH + (tk * 16u + (lane & 15u)) * FSTR, tag, tag2);
    STAMP(10);
    const u32 hv = WS_H + tk * 86u * 1024u + lane * 16u;
    const u32 st0 = nb * 5u;
    auto kstep = [&](int i) -> u32 {
      if (i < 5) return 76u + kh * 5u + i;
      u32 k = st0 + (i - 5); k = k >= 38u ? k - 38u : k;
      return kh * 38u + k;
    };
    v4u act[43];
#pragma unroll
    for (int i = 0; i < WIN; ++i) act[i] = __builtin_amdgcn_raw_buffer_load_b128(rsW, hv, kstep(i) * 1024u, 16);
    xres = *(const v4u*)(p.x + xo);
    FENCE();
    BAR();
    STAMP(11);
    v4u n0, n1;
#pragma unroll
    for (int i = 0; i < 43; ++i) {
      v4u c0, c1;
      if (i < 5) { c0 = wr[2 * i]; c1 = wr[2 * i + 1]; } else { c0 = n0; c1 = n1; }
      if (i + 1 >= 5 && i + 1 < 43) { const u32 a = kstep(i + 1) * 2048u + pa0; n0 = ldsfrag(a, 1024u); n1 = ldsfrag(a + 256u, 1024u); }
      FENCE();
      d0 = mfma32(c0, act[i], d0); d1 = mfma32(c1, act[i], d1);
      if (i + WIN < 43) act[i + WIN] = __builtin_amdgcn_raw_buffer_load_b128(rsW, hv, kstep(i + WIN) * 1024u, 16);
      FENCE();
    }
  } else {
    BAR_VM(0);
    STAMP(11);
    xres = *(const v4u*)(p.x + xo);
    if (b == 0u && w == 7u) {
      wait_flags(rsW, WS_FH + ((lane >> 3) * 64u + (lane & 7u) * 2u) * FSTR, tag, tag2);
      wait_flags(rsW, WS_FH + ((lane >> 3) * 64u + 512u + (lane & 7u) * 2u) * FSTR, tag, tag2);
    }
    wait_flags(rsW, WS_FO + (tk * 16u + kh * 8u + (lane & 7u)) * FSTR, tag, tag2);
    STAMP(10);
    const u32 ov = WS_O + (tk * 32u + kh * 16u) * 1024u + lane * 16u;
    v4u act[16];
#pragma unroll
    for (u32 i = 0; i < 16; ++i) act[i] = __builtin_amdgcn_raw_buffer_load_b128(rsW, ov, ((i + 2u * nb) & 15u) * 1024u, 16);
    FENCE();
#pragma unroll
    for (u32 i = 0; i < 16; ++i) { d0 = mfma32(wr[2 * i], act[i], d0); d1 = mfma32(wr[2 * i + 1], act[i], d1); }
  }
  STAMP(12);
  BAR();
  if (b == 0u && w == 7u) __builtin_amdgcn_raw_buffer_store_b32(tag, rsW, lane == 0u ? WS_E : 0x80000000u, 0, 16);
  {
    const u32 a = (hf * 2u + kh) * 17408u + (32u * mrow + l31) * 272u + hh * 16u;
#pragma unroll
    for (int g = 0; g < 4; ++g) {
      *(LAS f4*)(a + g * 32u) = (f4){d0[4 * g], d0[4 * g + 1], d0[4 * g + 2], d0[4 * g + 3]};
      *(LAS f4*)(a + 128u + g * 32u) = (f4){d1[4 * g], d1[4 * g + 1], d1[4 * g + 2], d1[4 * g + 3]};
    }
  }
  BAR();
  STAMP(13);
  {
    const u32 a = (tid >> 3) * 272u + (tid & 7u) * 32u;
    const v4u bb = lds16(SM_B2 + nb * 128u + (tid & 7u) * 16u);
    float res[8];
#pragma unroll
    for (int hx = 0; hx < 2; ++hx) {
      const f4 m0 = *(LAS f4*)(a + hx * 16u), m1 = *(LAS f4*)(a + 17408u + hx * 16u);
      const f4 s0 = *(LAS f4*)(a + 34816u + hx * 16u), s1 = *(LAS f4*)(a + 52224u + hx * 16u);
#pragma unroll
      for (int j = 0; j < 4; ++j) {
        const u32 bw = bb[hx * 2 + (j >> 1)], xw = xres[hx * 2 + (j >> 1)];
        const float m = (m0[j] + m1[j]) + ((j & 1) ? bhi(bw) : blo(bw));
        const float g = m * __builtin_amdgcn_rcpf(1.f + __builtin_amdgcn_exp2f(m * -1.4426950409f));
        res[hx * 4 + j] = __builtin_fmaf(s0[j] + s1[j], g, (j & 1) ? bhi(xw) : blo(xw));
      }
    }
    *(v4u*)(p.out + xo) = (v4u){pk(res[0], res[1]), pk(res[2], res[3]), pk(res[4], res[5]), pk(res[6], res[7])};
  }
  STAMP(14);
  };
  auto finish = [&]() {
    asm volatile("s_endpgm" : "=v"(xr[0]), "=v"(xr[1]), "=v"(xr[2]), "=v"(xr[3]), "=v"(xr[4]), "=v"(xr[5]), "=v"(xr[6]), "=v"(xr[7]),
                 "=v"(xr[8]), "=v"(xr[9]), "=v"(xr[10]), "=v"(xr[11]), "=v"(xr[12]), "=v"(xr[13]), "=v"(xr[14]), "=v"(xr[15]), "=v"(acc0), "=v"(acc1) :: "memory");
    asm volatile("" : "=v"(xr[16]), "=v"(xr[17]), "=v"(xr[18]), "=v"(xr[19]), "=v"(xr[20]), "=v"(xr[21]), "=v"(xr[22]), "=v"(xr[23]),
                 "=v"(xr[24]), "=v"(xr[25]), "=v"(xr[26]), "=v"(xr[27]), "=v"(xr[28]), "=v"(xr[29]), "=v"(xr[30]), "=v"(xr[31]), "=v"(tidv) :: "memory");
  };
  u32 role = isM * 2u + hf;
  if (role == 0u) { tail(IC<0>{}, IC<0>{}); finish(); }
  asm volatile("" : "+s"(role));
  if (role == 1u) { tail(IC<0>{}, IC<1>{}); finish(); }
  asm volatile("" : "+s"(role));
  if (role == 2u) { tail(IC<1>{}, IC<0>{}); finish(); }
  tail(IC<1>{}, IC<1>{});
  asm volatile(".Lhh_end:");
}

__global__ void hh_null(char* p) {
  u64 rt0; asm volatile("s_memrealtime %0\n\ts_waitcnt lgkmcnt(0)" : "=s"(rt0) :: "memory");
  for (int sp_ = 0; sp_ < 100000; ++sp_) { u64 t_; asm volatile("s_memrealtime %0\n\ts_waitcnt lgkmcnt(0)" : "=s"(t_) :: "memory"); if (t_ - rt0 >= 2000ull) break; }
  if (p == nullptr) __builtin_trap(); }
extern "C" void kernel_launch(void* const* d_in, const int* in_sizes, int n_in,
                              void* d_out, int out_size, void* d_ws, size_t ws_size,
                              hipStream_t stream) {
  Params p;
  p.x = (const char*)d_in[0]; p.wqkv = (const char*)d_in[1]; p.wphi = (const char*)d_in[2]; p.wo = (const char*)d_in[3];
  p.w1 = (const char*)d_in[5]; p.b1 = (const char*)d_in[6]; p.w2 = (const char*)d_in[7]; p.b2 = (const char*)d_in[8];
  p.out = (char*)d_out; p.ws = (char*)d_ws;
  (void)hipFuncSetAttribute((const void*)hh_fused, hipFuncAttributeMaxDynamicSharedMemorySize, (int)LDS_BYTES);
  hh_fused<<<256, 512, LDS_BYTES, stream>>>(p);
  hh_null<<<1, 64, 0, stream>>>(p.ws);
}
```

```cpp
#include <hip/hip_runtime.h>
#include <hip/hip_bf16.h>
#pragma clang diagnostic ignored "-Wint-to-pointer-cast"

typedef unsigned u32;
typedef unsigned long long u64;
typedef u32 v4u __attribute__((ext_vector_type(4)));
typedef u32 v2u __attribute__((ext_vector_type(2)));
typedef float f2 __attribute__((ext_vector_type(2)));
typedef float f4 __attribute__((ext_vector_type(4)));
typedef float f16v __attribute__((ext_vector_type(16)));
typedef __bf16 bf2 __attribute__((ext_vector_type(2)));
typedef __bf16 bf8 __attribute__((ext_vector_type(8)));
typedef short s4 __attribute__((ext_vector_type(4)));
#define LAS __attribute__((address_space(3)))
#define DEV __device__ __forceinline__

#define FSTR 128u
#define WS_E 0u
#define WS_FKV 4096u
#define WS_FH (WS_FKV + 1024u * FSTR)
#define WS_FO (WS_FH + 1024u * FSTR)
#define WS_ZP (WS_FO + 1024u * FSTR)
#define WS_KVP (WS_ZP + 65536u)
#define WS_O (WS_KVP + 524288u)
#define WS_H (WS_O + 2097152u)
#define WS_ST (WS_H + 64u * 86u * 1024u)
#define WS_END (WS_ST + 65536u)
#define WS_STAMP (16u << 20)
#define SLOT(i) ((u32)(i) * 32768u)
#define IM_PHI 131072u
#define IM_V 147456u
#define SM_WB 155648u
#define SM_CS SM_WB
#define SM_KVT 158720u
#define SM_B2 163072u
#define SM_ZERO 163328u
#define SM_SYNC 163584u
#define LDS_BYTES 163840u
#define SPIN_CAP 60000
#define MWIN 9

#define STAMP(i)

template <int N> struct IC { static constexpr int v = N; };
template <int I, int N, class F> DEV void sfor(F&& f) { if constexpr (I < N) { f(IC<I>{}); sfor<I + 1, N>(f); } }
struct Params {
  const char* x; const char* wqkv; const char* w1; const char* wphi; const char* b1;
  const char* wo; const char* w2; const char* b2; char* out; char* ws;
};

DEV v4u mk_srd(const void* p, u32 bytes) {
  const u64 a = (u64)p;
  return (v4u){(u32)a, (u32)(a >> 32) & 0xffffu, bytes, 0x00020000u};
}
#define DMA_TXT(v, r, so, o) "buffer_load_dwordx4 " v ", " r ", " so " offen offset:" o " lds"
template <int OFF> DEV void dma(v4u srd, u32 voff, u32 soff) {
  asm volatile(DMA_TXT("%0", "%1", "%2", "%3") :: "v"(voff), "s"(srd), "s"(soff), "n"(OFF) : "memory");
}
template <u32 LDS> DEV void dma_nx(v4u srd, u32 voff, u32& kx, u32& kw, u32 whalf, u32 base) {
  asm volatile("s_add_u32 m0, %2, %3\n\ts_add_u32 %0, %0, 0x80\n\ts_and_b32 %0, %0, 0x3ff\n\t" DMA_TXT("%5", "%6", "%0", "0") "\n\ts_mul_i32 %1, %0, %4"
               : "+s"(kx), "=s"(kw) : "s"(base), "n"(LDS), "s"(whalf), "v"(voff), "s"(srd) : "scc", "memory");
}
template <u32 LDS> DEV void dma_nw(v4u srd, u32 voff, u32& kx, u32& kw, u32 whalf, u32 base) {
  asm volatile("s_add_u32 m0, %2, %3\n\ts_add_u32 %0, %0, 0x80\n\ts_and_b32 %0, %0, 0x3ff\n\ts_mul_i32 %1, %0, %4\n\t" DMA_TXT("%5", "%6", "%1", "0")
               : "+s"(kx), "=&s"(kw) : "s"(base), "n"(LDS), "s"(whalf), "v"(voff), "s"(srd) : "scc", "memory");
}
template <int E> DEV void dma_grp(v4u srd, u32 voff, u32& gs, u32 lds, u32 mem) {
  if constexpr (E == 0) asm volatile("s_mov_b32 m0, %1\n\ts_mov_b32 %0, %2\n\t" DMA_TXT("%3", "%4", "%0", "0") : "=&s"(gs) : "s"(lds), "s"(mem), "v"(voff), "s"(srd) : "memory");
  else asm volatile("s_add_u32 %0, %0, 0x1c00\n\t" DMA_TXT("%1", "%2", "%0", "%3") : "+s"(gs) : "v"(voff), "s"(srd), "n"(E * 1024) : "scc", "memory");
}
DEV void ld16_sc1(v4u& d, v4u srd, u32 voff) { asm volatile("buffer_load_dwordx4 %0, %1, %2, 0 offen sc1" : "=v"(d) : "v"(voff), "s"(srd) : "memory"); }
DEV void hold(v4u a, v4u c) { asm volatile("s_nop 1" :: "v"(a), "v"(c) : "memory"); }
template <class T> DEV void pin(T& v) { asm volatile("" : "+v"(v)); }
DEV void dma16(v4u srd, u32 voff, u32 dst) {
  asm volatile("s_mov_b32 m0, %0\n\ts_nop 0\n\tbuffer_load_dwordx4 %1, %2, 0 offen lds" :: "s"(dst), "v"(voff), "s"(srd) : "memory");
}
DEV void dma4(v4u srd, u32 voff, u32 dst) {
  asm volatile("s_mov_b32 m0, %0\n\ts_nop 0\n\tbuffer_load_dword %1, %2, 0 offen lds" :: "s"(dst), "v"(voff), "s"(srd) : "memory");
}
#define WAIT_VM(n) asm volatile("s_waitcnt vmcnt(" #n ")" ::: "memory")
#define BAR_VM(n) asm volatile("s_waitcnt vmcnt(" #n ") lgkmcnt(0)\n\ts_barrier" ::: "memory")
#define BAR() asm volatile("s_waitcnt lgkmcnt(0)\n\ts_barrier" ::: "memory")
#define FENCE() __builtin_amdgcn_sched_barrier(0)
#define OPQ(x) pin(x)

DEV v4u lds16(u32 a) { return *(LAS v4u*)a; }
DEV v2u ldstr(u32 a) { return __builtin_bit_cast(v2u, __builtin_amdgcn_ds_read_tr16_b64_v4i16((LAS s4*)a)); }
DEV v4u ldsfrag(u32 a, u32 d) { const v2u p = ldstr(a), q = ldstr(a + d); return (v4u){p.x, p.y, q.x, q.y}; }
DEV f16v mfma32(v4u a, v4u b, f16v c) { f16v d = __builtin_amdgcn_mfma_f32_32x32x16_bf16(__builtin_bit_cast(bf8, a), __builtin_bit_cast(bf8, b), c, 0, 0, 0); OPQ(d); return d; }
DEV f4 mfma16(v4u a, v4u b, f4 c) { f4 d = __builtin_amdgcn_mfma_f32_16x16x32_bf16(__builtin_bit_cast(bf8, a), __builtin_bit_cast(bf8, b), c, 0, 0, 0); OPQ(d); return d; }
DEV u32 pk(float a, float b) { return __builtin_bit_cast(u32, __builtin_convertvector((f2){a, b}, bf2)); }
DEV float blo(u32 v) { return __uint_as_float(v << 16); }
DEV float bhi(u32 v) { return __uint_as_float(v & 0xffff0000u); }
DEV v4u pk8(const f16v& a, int o) { return (v4u){pk(a[o], a[o + 1]), pk(a[o + 2], a[o + 3]), pk(a[o + 4], a[o + 5]), pk(a[o + 6], a[o + 7])}; }
extern "C" __device__ float mul1(float, float) __asm("llvm.amdgcn.fmul.legacy");

DEV u32 opq(u32 v) { OPQ(v); return v; }
DEV u32 split2(float v) { return pk(v, v - bhi(pk(v, v))); }
DEV v4u fold_w(f4 c, u32 bb) {
  const float cs = ((c[0] + c[1]) + c[2]) + c[3];
  const u32 h = pk(cs, cs);
  const float l = cs - bhi(h);
  return (v4u){h, pk(l, l), bb, 0u};
}
DEV float wave_sum(float v) {
  v += __uint_as_float(__builtin_amdgcn_update_dpp(0u, __float_as_uint(v), 0x121, 0xf, 0xf, false));
  v += __uint_as_float(__builtin_amdgcn_update_dpp(0u, __float_as_uint(v), 0x122, 0xf, 0xf, false));
  v += __uint_as_float(__builtin_amdgcn_update_dpp(0u, __float_as_uint(v), 0x124, 0xf, 0xf, false));
  v += __uint_as_float(__builtin_amdgcn_update_dpp(0u, __float_as_uint(v), 0x128, 0xf, 0xf, false));
  v2u s2 = __builtin_amdgcn_permlane16_swap(__float_as_uint(v), __float_as_uint(v), false, false);
  v = __uint_as_float(s2.x) + __uint_as_float(s2.y);
  s2 = __builtin_amdgcn_permlane32_swap(__float_as_uint(v), __float_as_uint(v), false, false);
  return __uint_as_float(s2.x) + __uint_as_float(s2.y);
}
template <class H> DEV void softmax16(const f16v& p, float c, v4u& o0, v4u& o1, H&& hook) {
  float mx = fmaxf(fmaxf(p[0], p[1]), p[2]);
#pragma unroll
  for (int i = 3; i < 15; i += 2) mx = fmaxf(fmaxf(mx, p[i]), p[i + 1]);
  mx = fmaxf(mx, p[15]);
  const float nm = -mx * c;
  float e[16], s0 = 0.f;
  sfor<0, 4>([&](auto q) {
    hook(q);
#pragma unroll
    for (int i = 4 * q.v; i < 4 * q.v + 4; i += 2) {
      e[i] = __builtin_amdgcn_exp2f(__builtin_fmaf(p[i], c, nm));
      e[i + 1] = __builtin_amdgcn_exp2f(__builtin_fmaf(p[i + 1], c, nm));
      s0 = i ? (s0 + e[i]) + e[i + 1] : e[i] + e[i + 1];
    }
    OPQ(s0);
  });
  const float rs = __builtin_amdgcn_rcpf(s0);
#pragma unroll
  for (int i = 0; i < 4; ++i) { o0[i] = pk(mul1(e[2 * i], rs), mul1(e[2 * i + 1], rs)); o1[i] = pk(mul1(e[8 + 2 * i], rs), mul1(e[9 + 2 * i], rs)); }
  asm volatile("" : "+v"(o0), "+v"(o1));
}
template <class H> DEV void gelu16(const f16v& a, float c0, float c1, float sig, v4u& o0, v4u& o1, H&& hook) {
  sfor<0, 4>([&](auto q) {
    hook(q);
    float g[4];
#pragma unroll
    for (int j = 0; j < 4; ++j) {
      const float v = a[4 * q.v + j];
      const float e = __builtin_amdgcn_exp2f(v * __builtin_fmaf(v * v, c1, c0));
      g[j] = mul1(v, __builtin_amdgcn_rcpf(__builtin_fmaf(e, sig, sig)));
    }
    v4u& o = q.v < 2 ? o0 : o1;
    u32 pa = pk(g[0], g[1]), pb = pk(g[2], g[3]);
    asm volatile("" : "+v"(pa), "+v"(pb));
    o[(q.v & 1) * 2] = pa; o[(q.v & 1) * 2 + 1] = pb;
  });
}
DEV void wait_flags(__amdgpu_buffer_rsrc_t rs, u32 voff, u32 tag, u32 tag2) {
#pragma unroll 1
  for (int sp = 0; sp < SPIN_CAP; ++sp) {
    const v2u f = __builtin_amdgcn_raw_buffer_load_b64(rs, voff, 0, 16);
    if (__all(f.x == tag && f.y == tag2)) break;
    asm volatile("s_sleep 2" ::: "memory");
  }
}
DEV void lds_arrive(u32 a) { asm volatile("s_waitcnt lgkmcnt(0)" ::: "memory"); *(volatile LAS u32*)a = 1u; }
DEV void lds_wait(u32 a) {
#pragma unroll 1
  for (int sp = 0; sp < 400000; ++sp) {
    const v4u f = *(volatile LAS v4u*)a;
    if (__builtin_amdgcn_readfirstlane(f.x & f.y & f.z & f.w)) break;
    asm volatile("s_sleep 1" ::: "memory");
  }
  asm volatile("" ::: "memory");
}

__global__ void __launch_bounds__(512, 2) hh_fused(Params p) {
  const u32 tid = threadIdx.x, lane = tid & 63u;
  const u32 w = __builtin_amdgcn_readfirstlane(tid >> 6);
  u32 b = blockIdx.x;
  u64 px0, pw0, pws0, px, pw, pws;
  asm volatile("s_load_dwordx2 %0, %4, 0x0\n\ts_load_dwordx2 %1, %4, %5\n\ts_load_dwordx2 %2, %4, 0x48" : "=&s"(px0), "=&s"(pw0), "=&s"(pws0), "+s"(b)
               : "s"(__builtin_amdgcn_kernarg_segment_ptr()), "s"(8u + ((b >> 3) & 8u)));
  u32 pfd;
  {
    u64 pc; u32 len;
    asm volatile("s_getpc_b64 %0\n.Lhh_pc:\n\ts_mov_b32 %1, .Lhh_end-.Lhh_pc" : "=s"(pc), "=s"(len));
    const u32 vo = (lane >> 1) * 65536u + (lane & 1u) * 128u;
    asm volatile("buffer_load_dword %0, %1, %2, %3 offen" : "=v"(pfd) : "v"(vo), "s"(mk_srd((const void*)pc, len)), "s"(((b & ~7u) + w) * 256u) : "memory");
    asm volatile("" : "+s"(b));
  }
  const u32 xcd = b & 7u, rb = (b >> 3) & 31u, xg = xcd >> 1, gl = rb >> 3;
  const u32 isM = gl & 1u;
  const u32 idx = xg * 2u + (gl >> 1);
  const u32 t = (xcd & 1u) * 8u + (rb & 7u);
  const u32 koff = ((rb & 7u) + 2u * gl) & 7u;
  const u32 r = w & 3u, hf = w >> 2;
  const u32 hh = lane >> 5, l31 = lane & 31u, q4 = lane & 3u, rr = (lane >> 2) & 3u, g1 = (lane >> 4) & 1u;
  const u32 seg0 = 5u * idx + (idx < 3u ? idx : 3u);
  const u32 six = idx < 3u;
  const u32 whalf = isM ? 1376u : 1536u, wstr = 2u * whalf;
  const u32 row7 = (lane >> 2) & 7u;
  u32 vx0 = (16u * w + row7) * 1024u + hh * 64u + 16u * (q4 ^ (row7 >> 2));
  u32 vx1 = ((vx0 + 8192u) ^ 32u) - 1024u;
  const u32 colb = isM ? seg0 * 64u : 1024u + idx * 128u;
  u32 vw0 = (8u * w + rr) * wstr + colb + g1 * 64u + hh * (isM ? 128u : 1024u) + 16u * q4 + 2048u;
  u32 vw1 = vw0 + 4u * wstr - 1024u;
  const u32 w4k = w * 4096u;
  u32 kx = koff * 128u, kw = kx * whalf, xs = t * 131072u, wn = 512u * wstr + 4096u;
  const u32 srow = (idx * 2u + isM) * 8u + w;
  u32 vrow = srow * 1024u + lane * 16u, ep0, ep;
  asm volatile("" : "+v"(vx0), "+v"(vx1), "+v"(vw0), "+v"(vw1), "+v"(vrow));
  asm volatile("s_mov_b32 m0, %7\n\ts_waitcnt lgkmcnt(0)\n\ts_load_dword %3, %6, 0x0\n\ts_mov_b64 %0, %4\n\ts_mov_b64 %1, %5\n\ts_mov_b64 %2, %6"
               : "=&s"(px), "=&s"(pw), "=&s"(pws), "=&s"(ep0) : "s"(px0), "s"(pw0), "s"(pws0), "s"(w4k), "s"(xs), "s"(wn), "s"(kx), "s"(kw));
  const v4u srdX = mk_srd((const char*)px + xs, 131072u);
  v4u xrow;
  asm volatile("buffer_load_dwordx4 %0, %1, %2, 0 offen" : "=v"(xrow) : "v"(vrow), "s"(srdX) : "memory");
  const v4u srdW = mk_srd((const char*)pw - 4096, wn);
  u32 vu = (8u * w + 4u * hh + rr) * wstr + (isM ? seg0 * 64u + 256u + (six ? g1 * 64u : 0u) : idx * 128u + g1 * 64u) + 16u * q4 + 4096u;
  auto dq = [&](auto qc) {
    constexpr int P = qc.v >> 2, e = qc.v & 3;
    if constexpr (P < 8) {
      if constexpr (e == 0) {
        if constexpr (P > 0) dma_nx<SLOT(P & 3)>(srdX, vx0, kx, kw, whalf, w4k);
        else dma<0>(srdX, vx0, kx);
      } else if constexpr (e == 1) dma<2048>(srdW, vw0, kw);
      else if constexpr (e == 2) dma<1024>(srdX, vx1, kx);
      else dma<3072>(srdW, vw1, kw);
    } else {
      dma_nw<SLOT(P & 3) + e * 1024>(srdW, vu, kx, kw, whalf, w4k);
    }
  };
  sfor<0, 4>(dq);
  STAMP(1);
  const __attribute__((address_space(4))) Params* kp = (const __attribute__((address_space(4))) Params*)__builtin_amdgcn_kernarg_segment_ptr();
  asm volatile("s_barrier" : "+s"(kp) :: "memory");
  const u32 tb = (xcd & 1u) * 16u + xg * 4u + (rb & 3u), nb = rb >> 2;
  const __amdgpu_buffer_rsrc_t rsW = __builtin_amdgcn_make_buffer_rsrc((void*)pws, 0, WS_END, 0x00020000);
  asm volatile("s_waitcnt vmcnt(4) lgkmcnt(0)\n\ts_mov_b32 %0, %1" : "=s"(ep) : "s"(ep0) : "memory");
  asm volatile("" : "+v"(xrow), "+v"(pfd));
  const u32 tag = ep * 1664525u + 1013904223u, tag2 = (ep ^ 0x9e3779b9u) * 2654435761u + 12345u;
  {
    float xv[8];
#pragma unroll
    for (int i = 0; i < 4; ++i) { xv[2 * i] = blo(xrow[i]); xv[2 * i + 1] = bhi(xrow[i]); }
    const float mu = wave_sum(((xv[0] + xv[1]) + (xv[2] + xv[3])) + ((xv[4] + xv[5]) + (xv[6] + xv[7]))) * (1.f / 512.f);
    float q = 0.f;
#pragma unroll
    for (int i = 0; i < 8; ++i) q = __builtin_fmaf(xv[i] - mu, xv[i] - mu, q);
    const float var = wave_sum(q) * (1.f / 512.f);
    const float sg = __builtin_amdgcn_sqrtf(lane ? __builtin_fmaf(1e-5f, var + 1e-5f, var) : var + 1e-5f);
    __builtin_amdgcn_raw_buffer_store_b128((v4u){split2(-mu), tag, __float_as_uint(sg), tag2}, rsW,
                                           lane < 2u ? WS_ST + lane * 32768u + (t * 128u + srow) * 16u : 0x80000000u, 0, 16);
  }
  if (w >= 5u) {
    if (w == 7u) dma4(mk_srd(kp->b2, 1024u), nb * 128u + lane * 4u, SM_B2);
    else dma16(mk_srd(isM ? kp->b1 : kp->wphi, isM ? 2752u : 16384u), (isM ? seg0 * 64u : idx * 2048u + (w - 5u) * 1024u) + lane * 16u, SM_WB + (w - 5u) * 1024u);
  }
  sfor<4, 8>(dq);
  if (tid < 16u) *(LAS u32*)(SM_SYNC + tid * 4u) = 0u;
  v4u srdW2 = mk_srd(kp->w2, 1376u * 1024u), srdWo = mk_srd(kp->wo, 512u * 1024u);
  char* pout = kp->out;
  asm volatile("" : "+s"(srdW2), "+s"(srdWo), "+s"(pout));
  OPQ(vu);
  u32 sma = opq(SM_WB + (isM ? hf * 64u + l31 * 2u : hf * 1024u + (4u * hh + rr) * 32u + (2u * g1 + (q4 >> 1)) * 8u));
  u32 vp = opq((4u * hh + rr) * 1024u + nb * 128u + g1 * 64u + 16u * q4);
  u32 csr = opq(hh ? SM_ZERO : SM_CS + (hf * 32u + l31) * 48u), csw = opq(csr + r * 4u);
  u32 vst = opq(WS_ST + isM * 32768u + (t * 128u + r * 32u + l31) * 16u);
  const u32 mrow = w & 1u, kh = (w >> 1) & 1u;
  u32 pa0 = opq(hh * 512u + rr * 64u + g1 * 32u + q4 * 8u + (hf ? SLOT(2) + kh * 32768u : kh * 10240u));
  const u32 tok32 = 4u * t + r;
  const u32 un = (r >> 1) * 4u + 2u * hf + (r & 1u);
  const u32 pi = (idx * 8u + un) * 16u + t;
  u32 ad[7];
  {
    const u32 sw = (l31 >> 2) & 1u, tk32 = (32u * r + l31) * 32u;
    const u32 la = (16u * hh + 4u * g1 + rr) * 32u + ((q4 * 8u) ^ (g1 * 16u));
    ad[0] = opq(isM ? WS_H + (tok32 * 86u + seg0 * 2u + hf * 2u) * 1024u + lane * 16u : WS_KVP + pi * 512u + lane * 8u);
    ad[1] = opq((lane & 15u) == 0u ? WS_ZP + pi * 64u + (lane >> 4) * 16u : 0x80000000u);
    ad[2] = opq(IM_PHI + hh * 4096u + tk32 + sw * 16u);
    ad[3] = opq(ad[2] ^ 16u);
    ad[4] = opq(IM_V + tk32 + (hh ^ sw) * 16u);
    ad[5] = opq(IM_PHI + ((r & 1u) * 2u + (r >> 1)) * 4096u + la);
    ad[6] = opq(IM_V + (r & 1u) * 4096u + la);
  }

  const u32 rot = r ^ (2u * hf);
  const u32 xa0 = (2u * r + g1) * 4096u + ((l31 >> 3) & 1u) * 1024u + (l31 & 7u) * 64u + ((16u * (hh ^ ((l31 >> 2) & 3u))) ^ ((rot & 1u) * 32u)) + (rot >> 1) * 512u;
  const u32 wl = hf * 256u + rr * 64u + g1 * 32u + q4 * 8u;
  const u32 wa0 = rot * 8192u + hh * 4096u + wl;
  u32 xa[4], wa[4];
#pragma unroll
  for (u32 s = 0; s < 4; ++s) { xa[s] = xa0 ^ ((s >> 1) * 512u + (s & 1u) * 32u); wa[s] = wa0 ^ (s * 8192u); }
  const u32 one2 = 0x3f803f80u;
  const u32 selv = ((lane & 15u) == (g1 ? 4u : 0u)) ? one2 : 0u;
  v4u sel = {selv, selv, selv, selv};
  asm volatile("" : "+v"(xa[0]), "+v"(xa[1]), "+v"(xa[2]), "+v"(xa[3]), "+v"(wa[0]), "+v"(wa[1]), "+v"(wa[2]), "+v"(wa[3]), "+v"(sel));

  v4u xr[32];
  v2u wf[16];
  f16v acc0, acc1;
  f4 cs0 = {0.f, 0.f, 0.f, 0.f}, cs1 = {0.f, 0.f, 0.f, 0.f};
#pragma unroll
  for (int i = 0; i < 16; ++i) { acc0[i] = 0.f; acc1[i] = 0.f; }
  v4u aphi[2], rec, fx;
  float rinv, sig;
  auto frag = [&](int s, int u) { const v2u a = wf[s * 4 + u * 2], c = wf[s * 4 + u * 2 + 1]; return (v4u){a.x, a.y, c.x, c.y}; };
  sfor<0, 8>([&](auto cpc) {
    constexpr int cp = cpc.v;
    constexpr u32 sl = SLOT(cp & 1);
    auto rx = [&](int s) { xr[cp * 4 + s] = lds16(xa[s] + sl); };
    auto rw = [&](int s, int u, int h) { wf[s * 4 + u * 2 + h] = ldstr(wa[s] + sl + 2048u + u * 512u + h * 1024u); };
    if constexpr (cp == 5) { BAR_VM(5); } else { BAR_VM(4); }
    if constexpr (cp == 0) STAMP(2);
    auto flip = [&](int s) {
      if constexpr (cp & 1) { wa[s] = opq(wa[s] ^ 65536u); if constexpr (cp < 7) xa[s] = opq(xa[s] ^ 65536u); }
    };
    FENCE();
    if constexpr (cp > 0) acc0 = mfma32(frag(2, 0), xr[cp * 4 - 2], acc0);
    FENCE();
    rx(0); rw(0, 0, 0); rw(0, 0, 1);
    FENCE();
    if constexpr (cp > 0) acc1 = mfma32(frag(2, 1), xr[cp * 4 - 2], acc1);
    FENCE();
    rw(0, 1, 0); rw(0, 1, 1); rx(1);
    if constexpr (cp > 0) { FENCE(); dq(IC<cp * 4 + 8>{}); }
    FENCE();
    if constexpr (cp > 0) acc0 = mfma32(frag(3, 0), xr[cp * 4 - 1], acc0);
    FENCE();
    rw(1, 0, 0); rw(1, 0, 1); rw(1, 1, 0);
    flip(0);
    FENCE();
    if constexpr (cp > 0) acc1 = mfma32(frag(3, 1), xr[cp * 4 - 1], acc1);
    FENCE();
    rw(1, 1, 1); rw(2, 0, 0); rw(2, 0, 1); rx(2);
    if constexpr (cp > 0) { FENCE(); dq(IC<cp * 4 + 9>{}); }
    FENCE();
    if constexpr (cp == 7) { cs0 = mfma16(sel, frag(0, 0), cs0); cs1 = mfma16(sel, frag(0, 1), cs1); }
    acc0 = mfma32(frag(0, 0), xr[cp * 4], acc0);
    FENCE();
    rw(2, 1, 0); rw(2, 1, 1); rx(3);
    flip(1);
    if constexpr (cp == 6) {
      OPQ(rec);
      if (!__all(rec[1] == tag && rec[3] == tag2)) {
#pragma unroll 1
        for (int sp = 0; sp < SPIN_CAP; ++sp) {
          rec = __builtin_amdgcn_raw_buffer_load_b128(rsW, vst, 0, 16);
          if (__all(rec[1] == tag && rec[3] == tag2)) break;
          asm volatile("s_sleep 2" ::: "memory");
        }
      }
    }
    FENCE();
    acc1 = mfma32(frag(0, 1), xr[cp * 4], acc1);
    FENCE();
    rw(3, 0, 0); rw(3, 0, 1); rw(3, 1, 0); rw(3, 1, 1);
    if constexpr (cp > 0) { FENCE(); dq(IC<cp * 4 + 10>{}); }
    FENCE();
    if constexpr (cp < 7) { cs0 = mfma16(sel, frag(0, 0), cs0); cs1 = mfma16(sel, frag(0, 1), cs1); }
    FENCE();
    flip(2);
    if constexpr (cp == 6) { sig = __uint_as_float(rec[2]); rinv = __builtin_amdgcn_rcpf(sig); OPQ(rinv); }
    if constexpr (cp == 7) { *(LAS float*)(csw) = cs0[0]; *(LAS float*)(csw + 16u) = cs1[0]; }
    FENCE();
    acc0 = mfma32(frag(1, 0), xr[cp * 4 + 1], acc0);
    FENCE();
    flip(3);
    if constexpr (cp == 6) { fx = (v4u){rec[0], rec[0], split2(sig), 0u}; OPQ(fx); }
    if constexpr (cp > 0) { FENCE(); dq(IC<cp * 4 + 11>{}); }
    if constexpr (cp == 4) ld16_sc1(rec, mk_srd((const void*)pws, WS_END), vst);
    FENCE();
    acc1 = mfma32(frag(1, 1), xr[cp * 4 + 1], acc1);
    if constexpr (cp == 0) {
      FENCE();
      if (isM) {
        const u32 bA = *(LAS unsigned short*)sma, bB = *(LAS unsigned short*)(sma + 128u), bC = *(LAS unsigned short*)(sma + (six ? 256u : 192u));
        const u32 m = hh ? 0u : 0x10001u;
        aphi[0] = (v4u){bA * m, bB * m, bC * m, 0u}; aphi[1] = (v4u){0u, 0u, 0u, 0u};
      } else {
        const u32 sgn = (lane & 4u) ? 0x80008000u : 0u;
#pragma unroll
        for (u32 e = 0; e < 2; ++e) {
          aphi[e] = ldsfrag(sma + e * 512u, 256u);
          aphi[e] ^= (v4u){sgn, sgn, sgn, sgn};
        }
      }
      OPQ(aphi[0]); OPQ(aphi[1]);
      FENCE();
      sfor<8, 12>(dq);
    }
    FENCE();
  });
  BAR_VM(4);
  STAMP(3);
  u32 gs;
  auto wdma = [&](auto ec, v4u srd, u32 lds, u32 mem) {
    FENCE();
    dma_grp<ec.v>(srd, vp, gs, lds, mem);
    FENCE();
  };
  {
    FENCE();
    acc0 = mfma32(frag(2, 0), xr[30], acc0);
    FENCE();
    const f4 ka = *(LAS f4*)(csr), kb = *(LAS f4*)(csr + 16u);
    wdma(IC<0>{}, srdWo, SLOT(2) + w4k, w4k * 8u);
    acc0 = mfma32(frag(3, 0), xr[31], acc0);
    wdma(IC<1>{}, srdWo, 0u, 0u);
    acc1 = mfma32(frag(2, 1), xr[30], acc1);
    FENCE();
    v4u fa = fold_w(ka, isM ? aphi[0][0] : 0u);
    OPQ(fa);
    wdma(IC<2>{}, srdWo, 0u, 0u);
    acc0 = mfma32(fa, fx, acc0);
    FENCE();
    v4u fb = fold_w(kb, isM ? aphi[0][1] : 0u);
    OPQ(fb);
    FENCE();
    acc1 = mfma32(frag(3, 1), xr[31], acc1);
    wdma(IC<3>{}, srdWo, 0u, 0u);
    acc1 = mfma32(fb, fx, acc1);
    FENCE();
  }
  u32 tidv = tid;
  OPQ(tidv);
  auto tail = [&](auto ismc, auto hfc) {
  constexpr bool ISM = decltype(ismc)::v; constexpr u32 hf = decltype(hfc)::v;
  const u32 tid = tidv, lane = tid & 63u;
  const u32 hh = lane >> 5, l31 = lane & 31u, q4 = lane & 3u, rr = (lane >> 2) & 3u, g1 = (lane >> 4) & 1u;
  auto nohook = [&](auto) {};
  auto wgrp = [&](v4u srd, u32 lds, u32 mem) { sfor<0, 4>([&](auto e) { wdma(e, srd, lds, mem); }); };
  auto w2grp = [&](u32 g) { wgrp(srdW2, g * 4096u, g * 32768u); };
  auto hk_wo1 = [&](auto e) { wdma(e, srdWo, SLOT(3) + w4k, w4k * 8u + 262144u); };
  auto hk_s0 = [&](auto e) { wdma(e, srdW2, w4k, w4k * 8u + (w < 5u ? 1216u * 1024u : 0u)); };
  auto hk_s2 = [&](auto e) { wdma(e, srdW2, SLOT(2) + w4k, (SLOT(2) + w4k) * 8u); };

  auto put_flag = [&](u32 off) {
    __builtin_amdgcn_raw_buffer_store_b64((v2u){tag, tag2}, rsW, lane == 0u ? off : 0x80000000u, 0, 16);
  };
  f16v acc2;
  f4 cs2 = {0.f, 0.f, 0.f, 0.f};
#pragma unroll
  for (int i = 0; i < 16; ++i) acc2[i] = 0.f;
  auto g2 = [&](auto i0c, auto i1c, auto&& hook) {
    constexpr int i0 = i0c.v, i1 = i1c.v;
    v4u u[i1 - i0];
    auto rd = [&](int i) { u[i - i0] = ldsfrag(wa[i & 3] + SLOT(i >> 4) + ((i >> 2) & 3) * 1024u, 512u); };
    rd(i0); rd(i0 + 1);
    sfor<i0, i1>([&](auto ic) {
      constexpr int i = ic.v;
      if constexpr (i + 2 < i1) rd(i + 2);
      FENCE();
      acc2 = mfma32(u[i - i0], xr[i], acc2);
      FENCE();
      if constexpr ((i & 3) == 0) cs2 = mfma16(sel, u[i - i0], cs2);
      hook(IC<i - i0>{});
    });
  };
  auto put_cs2 = [&]() { *(LAS float*)(csw + 32u) = cs2[0]; };
  auto fold2 = [&](u32 bb) { acc2 = mfma32(fold_w(*(LAS f4*)(csr + 32u), bb), fx, acc2); };

  v4u wr[32];
  auto to_regs = [&]() {
#pragma unroll
    for (u32 i = 0; i < (hf ? 16 : 5); ++i) { wr[2 * i] = ldsfrag(pa0 + i * 2048u, 1024u); wr[2 * i + 1] = ldsfrag(pa0 + i * 2048u + 256u, 1024u); }
  };
  auto st16 = [&](v4u v, u32 voff, u32 soff = 0u) { __builtin_amdgcn_raw_buffer_store_b128(v, rsW, voff, soff, 16); };
  auto st32 = [&](v4u a, v4u c, u32 voff, u32 soff) { st16(a, voff, soff); st16(c, voff + 1024u, soff); hold(a, c); };

  if constexpr (ISM) {
    const float c0 = -2.302208198f * rinv;
    const float c1 = -0.1029432397f * rinv * rinv * rinv;
    const u32 hb = ad[0], bC = aphi[0][2];
    v4u o0, o1;
    if constexpr (!hf) {
      gelu16(acc0, c0, c1, sig, o0, o1, hk_wo1);
      st32(o0, o1, hb, 0u);
      BAR_VM(10);
      STAMP(4);
      gelu16(acc1, c0, c1, sig, o0, o1, nohook);
      st32(o0, o1, hb, 4096u);
      if (six) g2(IC<0>{}, IC<16>{}, nohook);
      BAR_VM(4);
      STAMP(6);
      if (six) g2(IC<16>{}, IC<24>{}, nohook);
      BAR();
      STAMP(7);
      if (six) g2(IC<24>{}, IC<32>{}, [&](auto i) { if constexpr (i.v < 4) hk_s0(i); else hk_s2(IC<i.v - 4>{}); });
      else { sfor<0, 4>(hk_s0); sfor<0, 4>(hk_s2); }
      put_cs2();
      BAR_VM(4);
      STAMP(8);
      to_regs();
      BAR();
      STAMP(9);
      if (six) {
        fold2(bC);
        gelu16(acc2, c0, c1, sig, o0, o1, [&](auto e) { wdma(e, srdW2, (8u + r) * 4096u, (8u + r) * 32768u); });
        st32(o0, o1, hb, 8192u);
      } else w2grp(8u + r);
      STAMP(10);
      w2grp(12u + r); w2grp(r);
      WAIT_VM(8);
    } else {
      g2(IC<0>{}, IC<16>{}, [&](auto i) { if constexpr ((i.v & 3) == 1) hk_wo1(IC<(i.v >> 2)>{}); });
      BAR_VM(8);
      STAMP(4);
      g2(IC<16>{}, IC<32>{}, nohook);
      put_cs2();
      STAMP(5);
      gelu16(acc0, c0, c1, sig, o0, o1, nohook);
      st32(o0, o1, hb, 0u);
      BAR_VM(2);
      STAMP(6);
      to_regs();
      BAR();
      STAMP(7);
      gelu16(acc1, c0, c1, sig, o0, o1, [&](auto q) { if constexpr (q.v < 2) { hk_s0(IC<2 * q.v>{}); hk_s0(IC<2 * q.v + 1>{}); } else { hk_s2(IC<2 * q.v - 4>{}); hk_s2(IC<2 * q.v - 3>{}); } });
      st32(o0, o1, hb, 4096u);
      BAR_VM(6);
      STAMP(8);
      fold2(bC);
      BAR();
      STAMP(9);
      gelu16(acc2, c0, c1, sig, o0, o1, [&](auto e) { wdma(e, srdW2, (24u + r) * 4096u, (24u + r) * 32768u); });
      st32(o0, o1, hb, six ? 8192u : 6144u);
      STAMP(10);
      w2grp(28u + r); w2grp(32u + r);
      WAIT_VM(8);
    }
    put_flag(WS_FH + (tok32 * 16u + idx * 2u + hf) * FSTR);
    STAMP(11);
    if constexpr (hf) { if (r < 3u) w2grp(r == 0u ? 4u : 35u + r); }
  } else {
    const float cl = rinv * 1.4426950409f;
    f16v zero;
#pragma unroll
    for (int i = 0; i < 16; ++i) zero[i] = 0.f;
    v4u o0, o1, vv[2];
    f16v pp[2];
    auto logits = [&](const f16v& a) {
      const v4u k0 = pk8(a, 0), k1 = pk8(a, 8);
      pp[0] = mfma32(aphi[0], k0, zero); pp[1] = mfma32(aphi[1], k1, zero);
      FENCE();
    };
    auto put_phi = [&](u32 e) { *(LAS v4u*)(ad[2] + e * 8192u) = o0; *(LAS v4u*)(ad[3] + e * 8192u) = o1; };
    auto scale_v = [&]() {
#pragma unroll
      for (int i = 0; i < 8; ++i) vv[i >> 2][i & 3] = pk(mul1(acc1[2 * i], rinv), mul1(acc1[2 * i + 1], rinv));
      asm volatile("" : "+v"(vv[0]), "+v"(vv[1]));
      FENCE();
    };
    auto put_v = [&]() { *(LAS v4u*)(ad[4]) = vv[0]; *(LAS v4u*)(ad[4] + 4096u) = vv[1]; };
    v4u fa[4], fb[4];
    auto kv_read = [&]() {
#pragma unroll
      for (u32 ks = 0; ks < 4; ++ks) { fa[ks] = ldsfrag(ad[5] + ks * 1024u, 256u); fb[ks] = ldsfrag(ad[6] + ks * 1024u, 256u); }
    };
    auto kv_put = [&]() {
      f4 akv = {0.f, 0.f, 0.f, 0.f}, az = {0.f, 0.f, 0.f, 0.f};
      const v4u ones = {one2, one2, one2, one2};
#pragma unroll
      for (u32 ks = 0; ks < 4; ++ks) { akv = mfma16(fa[ks], fb[ks], akv); az = mfma16(fa[ks], ones, az); }
      __builtin_amdgcn_raw_buffer_store_b64((v2u){pk(akv[0], akv[1]), pk(akv[2], akv[3])}, rsW, ad[0], 0, 16);
      st16(__builtin_bit_cast(v4u, az), ad[1]);
    };
    v4u pv[8]; f4 z;
    auto gather_ld = [&]() {
      wait_flags(rsW, WS_FKV + ((idx * 8u + un) * 16u + (lane & 15u)) * FSTR, tag, tag2);
#pragma unroll
      for (u32 i = 0; i < 8; ++i) pv[i] = __builtin_amdgcn_raw_buffer_load_b128(rsW, lane * 16u + (i & 3u) * 1024u, WS_KVP + (idx * 8u + un) * 8192u + (i >> 2) * 4096u, 16);
      z = __builtin_bit_cast(f4, __builtin_amdgcn_raw_buffer_load_b128(rsW, lane * 16u, WS_ZP + (idx * 8u + un) * 1024u, 16));
      FENCE();
    };
    auto gather_sum = [&]() {
      float s8[8];
#pragma unroll
      for (int j = 0; j < 4; ++j) { s8[2 * j] = blo(pv[0][j]); s8[2 * j + 1] = bhi(pv[0][j]); }
#pragma unroll
      for (int i = 1; i < 8; ++i)
#pragma unroll
        for (int j = 0; j < 4; ++j) { s8[2 * j] += blo(pv[i][j]); s8[2 * j + 1] += bhi(pv[i][j]); }
      float tt[4];
#pragma unroll
      for (int j = 0; j < 4; ++j) {
        const v2u sw2 = __builtin_amdgcn_permlane32_swap(__float_as_uint(s8[j]), __float_as_uint(s8[4 + j]), false, false);
        tt[j] = __uint_as_float(sw2.x) + __uint_as_float(sw2.y);
      }
      const u32 c = ((2u * l31) & 15u) + hh, d = (c & 3u) + 8u * ((c >> 2) & 1u) + 4u * (c >> 3);
      *(LAS v2u*)(SM_KVT + un * 544u + d * 32u + (l31 >> 3) * 8u) = (v2u){pk(tt[0], tt[1]), pk(tt[2], tt[3])};
#pragma unroll
      for (int j = 0; j < 4; ++j) {
        float v = z[j];
        v += __uint_as_float(__builtin_amdgcn_update_dpp(0u, __float_as_uint(v), 0x124, 0xf, 0xf, false));
        v += __uint_as_float(__builtin_amdgcn_update_dpp(0u, __float_as_uint(v), 0x128, 0xf, 0xf, false));
        v2u s2 = __builtin_amdgcn_permlane16_swap(__float_as_uint(v), __float_as_uint(v), false, false);
        v = __uint_as_float(s2.x) + __uint_as_float(s2.y);
        s2 = __builtin_amdgcn_permlane32_swap(__float_as_uint(v), __float_as_uint(v), false, false);
        z[j] = __uint_as_float(s2.x) + __uint_as_float(s2.y);
      }
      if (lane < 4u) *(LAS v2u*)(SM_KVT + un * 544u + 512u + lane * 8u) = (v2u){pk(z[0], z[1]), pk(z[2], z[3])};
    };
    v4u phq[4];
    const u32 ka = opq(SM_KVT + (2u * hf + 4u * hh) * 544u + (l31 < 16u ? l31 : 16u) * 32u);
    auto put_o = [&]() {
      f16v d[2]; v4u ov[2];
      const v4u a0 = lds16(ka), a1 = lds16(ka + 544u), a2 = lds16(ka + 16u), a3 = lds16(ka + 560u);
      d[0] = mfma32(a0, phq[0], zero); d[1] = mfma32(a1, phq[2], zero);
      d[0] = mfma32(a2, phq[1], d[0]); d[1] = mfma32(a3, phq[3], d[1]);
#pragma unroll
      for (u32 e = 0; e < 2; ++e) {
        const float rd = __builtin_amdgcn_rcpf(d[e][8] + 1e-6f);
#pragma unroll
        for (int i = 0; i < 4; ++i) ov[e][i] = pk(mul1(d[e][2 * i], rd), mul1(d[e][2 * i + 1], rd));
      }
      st32(ov[0], ov[1], lane * 16u, WS_O + (tok32 * 32u + idx * 4u + 2u * hf) * 1024u);
    };
    if constexpr (!hf) {
      logits(acc0); scale_v(); put_v();
      softmax16(pp[0], cl, o0, o1, hk_wo1); put_phi(0);
      BAR_VM(8);
      STAMP(4);
      softmax16(pp[1], cl, o0, o1, nohook); put_phi(1);
      BAR();
      STAMP(5);
      kv_read(); kv_put();
      g2(IC<0>{}, IC<16>{}, nohook);
      BAR_VM(2);
      STAMP(6);
      g2(IC<16>{}, IC<24>{}, nohook);
      WAIT_VM(0);
      put_flag(WS_FKV + pi * FSTR);
      BAR();
      STAMP(7);
      g2(IC<24>{}, IC<32>{}, [&](auto i) { if constexpr (i.v < 4) hk_s0(i); else hk_s2(IC<i.v - 4>{}); });
      put_cs2();
      BAR_VM(4);
      STAMP(8);
      to_regs();
      fold2(0u);
      BAR();
      STAMP(9);
      logits(acc2);
      softmax16(pp[0], cl, phq[0], phq[1], nohook);
      gather_ld();
      STAMP(10);
      softmax16(pp[1], cl, phq[2], phq[3], [&](auto e) { wdma(e, srdW2, (8u + r) * 4096u, (8u + r) * 32768u); });
      gather_sum();
      BAR();
      STAMP(12);
      put_o();
      w2grp(12u + r);
      WAIT_VM(4);
      put_flag(WS_FO + (tok32 * 16u + idx * 2u) * FSTR);
      STAMP(14);
    } else {
      g2(IC<0>{}, IC<16>{}, [&](auto i) { if constexpr ((i.v & 3) == 1) hk_wo1(IC<(i.v >> 2)>{}); });
      BAR_VM(8);
      STAMP(4);
      g2(IC<16>{}, IC<32>{}, nohook);
      put_cs2();
      BAR();
      STAMP(5);
      logits(acc0); scale_v();
      softmax16(pp[0], cl, o0, o1, nohook);
      BAR_VM(0);
      STAMP(6);
      to_regs();
      put_phi(0); put_v();
      BAR();
      STAMP(7);
      softmax16(pp[1], cl, o0, o1, [&](auto q) { if constexpr (q.v < 2) { hk_s0(IC<2 * q.v>{}); hk_s0(IC<2 * q.v + 1>{}); } else { hk_s2(IC<2 * q.v - 4>{}); hk_s2(IC<2 * q.v - 3>{}); } });
      put_phi(1);
      BAR_VM(4);
      STAMP(8);
      kv_read();
      BAR();
      STAMP(9);
      kv_put();
      w2grp(24u + r); w2grp(28u + r);
      fold2(0u);
      WAIT_VM(8);
      put_flag(WS_FKV + pi * FSTR);
      STAMP(10);
      logits(acc2);
      softmax16(pp[0], cl, phq[0], phq[1], [&](auto e) { wdma(e, srdW2, r * 4096u, r * 32768u); });
      softmax16(pp[1], cl, phq[2], phq[3], [&](auto e) { wdma(e, srdW2, (32u + r) * 4096u, (32u + r) * 32768u); });
      if (r < 3u) w2grp(r == 0u ? 4u : 35u + r);
      STAMP(11);
      BAR();
      STAMP(12);
      BAR_VM(0);
      STAMP(16);
      gather_ld();
      STAMP(13);
      gather_sum();
      lds_arrive(SM_SYNC + r * 4u);
      lds_wait(SM_SYNC);
      put_o();
      WAIT_VM(0);
      put_flag(WS_FO + (tok32 * 16u + idx * 2u + 1u) * FSTR);
      STAMP(14);
    }
  }

  const u32 tk = 2u * tb + mrow;
  f16v d0, d1;
#pragma unroll
  for (int i = 0; i < 16; ++i) { d0[i] = 0.f; d1[i] = 0.f; }
  const u32 ra = opq((hf * 2u + kh) * 17408u + (32u * mrow + l31) * 272u + hh * 16u);
  auto put_part = [&]() {
#pragma unroll
    for (int g = 0; g < 4; ++g) {
      *(LAS f4*)(ra + g * 32u) = (f4){d0[4 * g], d0[4 * g + 1], d0[4 * g + 2], d0[4 * g + 3]};
      *(LAS f4*)(ra + 128u + g * 32u) = (f4){d1[4 * g], d1[4 * g + 1], d1[4 * g + 2], d1[4 * g + 3]};
    }
  };
  if constexpr (!hf) {
    constexpr int WIN = MWIN;
    const u32 hv = WS_H + (tk * 86u + kh * 38u) * 1024u;
    const u32 hs = (nb & 1u) * 19u;
    const u32 hvA = opq(hv + hs * 1024u + lane * 16u), hvB = opq(hv + (19u - hs) * 1024u + lane * 16u), hvT = opq(hv + (76u - kh * 33u) * 1024u + lane * 16u);
    const u32 laA = opq(pa0 + (kh * 33u + hs) * 2048u), laB = opq(pa0 + (kh * 33u + 19u - hs) * 2048u);
    const u32 xo = opq((64u * tb + (tid >> 3)) * 1024u + nb * 128u + (tid & 7u) * 16u);
    const u32 fa = opq((tid >> 3) * 272u + (tid & 7u) * 32u);
    BAR_VM(1);
    STAMP(16);
    wait_flags(rsW, WS_FH + (tk * 16u + (lane & 15u)) * FSTR, tag, tag2);
    STAMP(15);
    auto ldact = [&](int i) {
      const int j = i < 5 ? i : i < 24 ? i - 5 : i - 24;
      return __builtin_amdgcn_raw_buffer_load_b128(rsW, (i < 5 ? hvT : i < 24 ? hvA : hvB) + (j & 3) * 1024, (j >> 2) * 4096, 16);
    };
    const __amdgpu_buffer_rsrc_t rsX = __builtin_amdgcn_make_buffer_rsrc((void*)px, 0, 2097152, 0x00020000);
    v4u act[43], xres[2];
#pragma unroll
    for (int i = 0; i < WIN; ++i) act[i] = ldact(i);
    FENCE();
    v4u n0, n1;
#pragma unroll
    for (int i = 0; i < 43; ++i) {
      v4u c0, c1;
      if (i < 5) { c0 = wr[2 * i]; c1 = wr[2 * i + 1]; } else { c0 = n0; c1 = n1; }
      d0 = mfma32(c0, act[i], d0);
      FENCE();
      if (i + WIN < 43) act[i + WIN] = ldact(i + WIN);
      else if (i + WIN < 45) xres[i + WIN - 43] = __builtin_amdgcn_raw_buffer_load_b128(rsX, xo, (i + WIN - 43) * 32768, 0);
      if (i + 1 >= 5 && i + 1 < 43) { const u32 a = i + 1 < 24 ? laA + (i - 4) * 2048u : laB + (i - 23) * 2048u; n0 = ldsfrag(a, 1024u); n1 = ldsfrag(a + 256u, 1024u); }
      FENCE();
      d1 = mfma32(c1, act[i], d1);
      FENCE();
    }
    STAMP(17);
    lds_arrive(SM_SYNC + 16u + r * 4u);
    const v4u bb = lds16(SM_B2 + (tid & 7u) * 16u);
    lds_wait(SM_SYNC + 16u);
    put_part();
    lds_arrive(SM_SYNC + 32u + r * 4u);
    lds_wait(SM_SYNC + 32u);
    float gt[16], xf[16];
#pragma unroll
    for (int q = 0; q < 4; ++q) {
      const u32 a = fa + (q >> 1) * 8704u + (q & 1) * 16u;
      const f4 m0 = *(LAS f4*)(a), m1 = *(LAS f4*)(a + 17408u);
#pragma unroll
      for (int j = 0; j < 4; ++j) {
        const u32 bw = bb[(q & 1) * 2 + (j >> 1)], xw = xres[q >> 1][(q & 1) * 2 + (j >> 1)];
        const float m = (m0[j] + m1[j]) + ((j & 1) ? bhi(bw) : blo(bw));
        gt[q * 4 + j] = m * __builtin_amdgcn_rcpf(1.f + __builtin_amdgcn_exp2f(m * -1.4426950409f));
        xf[q * 4 + j] = (j & 1) ? bhi(xw) : blo(xw);
        asm volatile("" : "+v"(gt[q * 4 + j]), "+v"(xf[q * 4 + j]));
      }
    }
    STAMP(21);
    BAR();
    STAMP(18);
    const __amdgpu_buffer_rsrc_t rsO = __builtin_amdgcn_make_buffer_rsrc(pout, 0, 2097152, 0x00020000);
#pragma unroll
    for (int e = 0; e < 2; ++e) {
      float res[8];
#pragma unroll
      for (int hx = 0; hx < 2; ++hx) {
        const u32 a = fa + 34816u + e * 8704u + hx * 16u;
        const f4 s0 = *(LAS f4*)(a), s1 = *(LAS f4*)(a + 17408u);
#pragma unroll
        for (int j = 0; j < 4; ++j) res[hx * 4 + j] = __builtin_fmaf(s0[j] + s1[j], gt[e * 8 + hx * 4 + j], xf[e * 8 + hx * 4 + j]);
      }
      const v4u ov = {pk(res[0], res[1]), pk(res[2], res[3]), pk(res[4], res[5]), pk(res[6], res[7])};
      __builtin_amdgcn_raw_buffer_store_b128(ov, rsO, xo, e * 32768, 17);
      hold(ov, ov);
    }
  } else {
    if constexpr (ISM) { BAR_VM(0); STAMP(16); }
    if (b == 0u && w == 7u) {
      wait_flags(rsW, WS_FH + ((lane >> 3) * 64u + (lane & 7u) * 2u) * FSTR, tag, tag2);
      wait_flags(rsW, WS_FH + ((lane >> 3) * 64u + 512u + (lane & 7u) * 2u) * FSTR, tag, tag2);
    }
    const u32 ov = WS_O + (tk * 32u + kh * 16u) * 1024u;
#pragma unroll
    for (u32 ph = 0; ph < 2; ++ph) {
      wait_flags(rsW, WS_FO + (tk * 16u + kh * 8u + (lane & 3u) * 2u + ph) * FSTR, tag, tag2);
      if (ph) STAMP(15);
      v4u act[8];
#pragma unroll
      for (u32 i = 0; i < 8; ++i) act[i] = __builtin_amdgcn_raw_buffer_load_b128(rsW, lane * 16u + (ph * 2u + (i & 1u)) * 1024u, ov + (i >> 1) * 4096u, 16);
      FENCE();
      if (ph) lds_wait(SM_SYNC + 16u);
#pragma unroll
      for (u32 i = 0; i < 8; ++i) { const u32 k = (i >> 1) * 4u + ph * 2u + (i & 1u); d0 = mfma32(wr[2 * k], act[i], d0); d1 = mfma32(wr[2 * k + 1], act[i], d1); }
    }
    STAMP(17);
    put_part();
    BAR();
    STAMP(18);
    if (b == 0u && w == 7u) __builtin_amdgcn_raw_buffer_store_b32(tag, rsW, lane == 0u ? WS_E : 0x80000000u, 0, 16);
  }
  STAMP(19);
  };
  auto finish = [&]() {
    asm volatile("s_endpgm" : "=v"(xr[0]), "=v"(xr[1]), "=v"(xr[2]), "=v"(xr[3]), "=v"(xr[4]), "=v"(xr[5]), "=v"(xr[6]), "=v"(xr[7]),
                 "=v"(xr[8]), "=v"(xr[9]), "=v"(xr[10]), "=v"(xr[11]), "=v"(xr[12]), "=v"(xr[13]), "=v"(xr[14]), "=v"(xr[15]), "=v"(acc0), "=v"(acc1),
                 "=v"(fx), "=v"(rinv), "=v"(sig), "=v"(vp), "=v"(aphi[0]), "=v"(aphi[1]), "=v"(sel), "=v"(csr), "=v"(csw), "=v"(pa0) :: "memory");
    asm volatile("" : "=v"(xr[16]), "=v"(xr[17]), "=v"(xr[18]), "=v"(xr[19]), "=v"(xr[20]), "=v"(xr[21]), "=v"(xr[22]), "=v"(xr[23]),
                 "=v"(xr[24]), "=v"(xr[25]), "=v"(xr[26]), "=v"(xr[27]), "=v"(xr[28]), "=v"(xr[29]), "=v"(xr[30]), "=v"(xr[31]), "=v"(tidv),
                 "=v"(wa[0]), "=v"(wa[1]), "=v"(wa[2]), "=v"(wa[3]), "=v"(ad[0]), "=v"(ad[1]), "=v"(ad[2]), "=v"(ad[3]), "=v"(ad[4]), "=v"(ad[5]), "=v"(ad[6]) :: "memory");
  };
  u32 role = isM * 2u + hf;
  if (role == 0u) { tail(IC<0>{}, IC<0>{}); finish(); }
  asm volatile("" : "+s"(role));
  if (role == 1u) { tail(IC<0>{}, IC<1>{}); finish(); }
  asm volatile("" : "+s"(role));
  if (role == 2u) { tail(IC<1>{}, IC<0>{}); finish(); }
  tail(IC<1>{}, IC<1>{});
  asm volatile(".Lhh_end:");
}

extern "C" void kernel_launch(void* const* d_in, const int* in_sizes, int n_in,
                              void* d_out, int out_size, void* d_ws, size_t ws_size,
                              hipStream_t stream) {
  Params p;
  p.x = (const char*)d_in[0]; p.wqkv = (const char*)d_in[1]; p.wphi = (const char*)d_in[2]; p.wo = (const char*)d_in[3];
  p.w1 = (const char*)d_in[5]; p.b1 = (const char*)d_in[6]; p.w2 = (const char*)d_in[7]; p.b2 = (const char*)d_in[8];
  p.out = (char*)d_out; p.ws = (char*)d_ws;
  (void)hipFuncSetAttribute((const void*)hh_fused, hipFuncAttributeMaxDynamicSharedMemorySize, (int)LDS_BYTES);
  hh_fused<<<256, 512, LDS_BYTES, stream>>>(p);
}
```

```cpp
#include <hip/hip_runtime.h>
#include <hip/hip_bf16.h>
#pragma clang diagnostic ignored "-Wint-to-pointer-cast"

typedef unsigned u32;
typedef unsigned long long u64;
typedef u32 v4u __attribute__((ext_vector_type(4)));
typedef u32 v2u __attribute__((ext_vector_type(2)));
typedef float f2 __attribute__((ext_vector_type(2)));
typedef float f4 __attribute__((ext_vector_type(4)));
typedef float f16v __attribute__((ext_vector_type(16)));
typedef __bf16 bf2 __attribute__((ext_vector_type(2)));
typedef __bf16 bf8 __attribute__((ext_vector_type(8)));
typedef short s4 __attribute__((ext_vector_type(4)));
#define LAS __attribute__((address_space(3)))
#define DEV __device__ __forceinline__

#define FSTR 128u
#define WS_E 0u
#define WS_FKV 4096u
#define WS_FH (WS_FKV + 1024u * FSTR)
#define WS_FO (WS_FH + 1024u * FSTR)
#define WS_ZP (WS_FO + 1024u * FSTR)
#define WS_KVP (WS_ZP + 65536u)
#define WS_O (WS_KVP + 524288u)
#define WS_H (WS_O + 2097152u)
#define WS_ST (WS_H + 64u * 86u * 1024u)
#define WS_END (WS_ST + 65536u)
#define SLOT(i) ((u32)(i) * 32768u)
#define IM_PHI 131072u
#define IM_V 147456u
#define SM_WB 155648u
#define SM_CS SM_WB
#define SM_KVT 158720u
#define SM_B2 163072u
#define SM_ZERO 163328u
#define SM_SYNC 163584u
#define LDS_BYTES 163840u
#define SPIN_CAP 60000
#define MWIN 9

template <int N> struct IC { static constexpr int v = N; };
template <int I, int N, class F> DEV void sfor(F&& f) { if constexpr (I < N) { f(IC<I>{}); sfor<I + 1, N>(f); } }
struct Params {
  const char* x; const char* wqkv; const char* w1; const char* wphi; const char* b1;
  const char* wo; const char* w2; const char* b2; char* out; char* ws;
};

DEV v4u mk_srd(const void* p, u32 bytes) {
  const u64 a = (u64)p;
  return (v4u){(u32)a, (u32)(a >> 32) & 0xffffu, bytes, 0x00020000u};
}
#define DMA_TXT(v, r, so, o) "buffer_load_dwordx4 " v ", " r ", " so " offen offset:" o " lds"
template <int OFF> DEV void dma(v4u srd, u32 voff, u32 soff) {
  asm volatile(DMA_TXT("%0", "%1", "%2", "%3") :: "v"(voff), "s"(srd), "s"(soff), "n"(OFF) : "memory");
}
template <u32 LDS> DEV void dma_nx(v4u srd, u32 voff, u32& kx, u32& kw, u32 whalf, u32 base) {
  asm volatile("s_add_u32 m0, %2, %3\n\ts_add_u32 %0, %0, 0x80\n\ts_and_b32 %0, %0, 0x3ff\n\t" DMA_TXT("%5", "%6", "%0", "0") "\n\ts_mul_i32 %1, %0, %4"
               : "+s"(kx), "=s"(kw) : "s"(base), "n"(LDS), "s"(whalf), "v"(voff), "s"(srd) : "scc", "memory");
}
template <u32 LDS> DEV void dma_nw(v4u srd, u32 voff, u32& kx, u32& kw, u32 whalf, u32 base) {
  asm volatile("s_add_u32 m0, %2, %3\n\ts_add_u32 %0, %0, 0x80\n\ts_and_b32 %0, %0, 0x3ff\n\ts_mul_i32 %1, %0, %4\n\t" DMA_TXT("%5", "%6", "%1", "0")
               : "+s"(kx), "=&s"(kw) : "s"(base), "n"(LDS), "s"(whalf), "v"(voff), "s"(srd) : "scc", "memory");
}
template <int E> DEV void dma_grp(v4u srd, u32 voff, u32& gs, u32 lds, u32 mem) {
  if constexpr (E == 0) asm volatile("s_mov_b32 m0, %1\n\ts_mov_b32 %0, %2\n\t" DMA_TXT("%3", "%4", "%0", "0") : "=&s"(gs) : "s"(lds), "s"(mem), "v"(voff), "s"(srd) : "memory");
  else asm volatile("s_add_u32 %0, %0, 0x1c00\n\t" DMA_TXT("%1", "%2", "%0", "%3") : "+s"(gs) : "v"(voff), "s"(srd), "n"(E * 1024) : "scc", "memory");
}
DEV void ld16_sc1(v4u& d, v4u srd, u32 voff) { asm volatile("buffer_load_dwordx4 %0, %1, %2, 0 offen sc1" : "=v"(d) : "v"(voff), "s"(srd) : "memory"); }
DEV void hold(v4u a, v4u c) { asm volatile("s_nop 1" :: "v"(a), "v"(c) : "memory"); }
template <class T> DEV void pin(T& v) { asm volatile("" : "+v"(v)); }
DEV void dma16(v4u srd, u32 voff, u32 dst) {
  asm volatile("s_mov_b32 m0, %0\n\ts_nop 0\n\tbuffer_load_dwordx4 %1, %2, 0 offen lds" :: "s"(dst), "v"(voff), "s"(srd) : "memory");
}
DEV void dma4(v4u srd, u32 voff, u32 dst) {
  asm volatile("s_mov_b32 m0, %0\n\ts_nop 0\n\tbuffer_load_dword %1, %2, 0 offen lds" :: "s"(dst), "v"(voff), "s"(srd) : "memory");
}
#define WAIT_VM(n) asm volatile("s_waitcnt vmcnt(" #n ")" ::: "memory")
#define BAR_VM(n) asm volatile("s_waitcnt vmcnt(" #n ") lgkmcnt(0)\n\ts_barrier" ::: "memory")
#define BAR() asm volatile("s_waitcnt lgkmcnt(0)\n\ts_barrier" ::: "memory")
#define FENCE() __builtin_amdgcn_sched_barrier(0)
#define OPQ(x) pin(x)

DEV v4u lds16(u32 a) { return *(LAS v4u*)a; }
DEV v2u ldstr(u32 a) { return __builtin_bit_cast(v2u, __builtin_amdgcn_ds_read_tr16_b64_v4i16((LAS s4*)a)); }
DEV v4u ldsfrag(u32 a, u32 d) { const v2u p = ldstr(a), q = ldstr(a + d); return (v4u){p.x, p.y, q.x, q.y}; }
DEV f16v mfma32(v4u a, v4u b, f16v c) { f16v d = __builtin_amdgcn_mfma_f32_32x32x16_bf16(__builtin_bit_cast(bf8, a), __builtin_bit_cast(bf8, b), c, 0, 0, 0); OPQ(d); return d; }
DEV f4 mfma16(v4u a, v4u b, f4 c) { f4 d = __builtin_amdgcn_mfma_f32_16x16x32_bf16(__builtin_bit_cast(bf8, a), __builtin_bit_cast(bf8, b), c, 0, 0, 0); OPQ(d); return d; }
DEV u32 pk(float a, float b) { return __builtin_bit_cast(u32, __builtin_convertvector((f2){a, b}, bf2)); }
DEV float blo(u32 v) { return __uint_as_float(v << 16); }
DEV float bhi(u32 v) { return __uint_as_float(v & 0xffff0000u); }
DEV v4u pk8(const f16v& a, int o) { return (v4u){pk(a[o], a[o + 1]), pk(a[o + 2], a[o + 3]), pk(a[o + 4], a[o + 5]), pk(a[o + 6], a[o + 7])}; }
extern "C" __device__ float mul1(float, float) __asm("llvm.amdgcn.fmul.legacy");

DEV u32 opq(u32 v) { OPQ(v); return v; }
DEV u32 split2(float v) { return pk(v, v - bhi(pk(v, v))); }
DEV v4u fold_w(f4 c, u32 bb) {
  const float cs = ((c[0] + c[1]) + c[2]) + c[3];
  const u32 h = pk(cs, cs);
  const float l = cs - bhi(h);
  return (v4u){h, pk(l, l), bb, 0u};
}
DEV float wave_sum(float v) {
  v += __uint_as_float(__builtin_amdgcn_update_dpp(0u, __float_as_uint(v), 0x121, 0xf, 0xf, false));
  v += __uint_as_float(__builtin_amdgcn_update_dpp(0u, __float_as_uint(v), 0x122, 0xf, 0xf, false));
  v += __uint_as_float(__builtin_amdgcn_update_dpp(0u, __float_as_uint(v), 0x124, 0xf, 0xf, false));
  v += __uint_as_float(__builtin_amdgcn_update_dpp(0u, __float_as_uint(v), 0x128, 0xf, 0xf, false));
  v2u s2 = __builtin_amdgcn_permlane16_swap(__float_as_uint(v), __float_as_uint(v), false, false);
  v = __uint_as_float(s2.x) + __uint_as_float(s2.y);
  s2 = __builtin_amdgcn_permlane32_swap(__float_as_uint(v), __float_as_uint(v), false, false);
  return __uint_as_float(s2.x) + __uint_as_float(s2.y);
}
template <class H> DEV void softmax16(const f16v& p, float c, v4u& o0, v4u& o1, H&& hook) {
  float mx = fmaxf(fmaxf(p[0], p[1]), p[2]);
#pragma unroll
  for (int i = 3; i < 15; i += 2) mx = fmaxf(fmaxf(mx, p[i]), p[i + 1]);
  mx = fmaxf(mx, p[15]);
  const float nm = -mx * c;
  float e[16], s0 = 0.f;
  sfor<0, 4>([&](auto q) {
    hook(q);
#pragma unroll
    for (int i = 4 * q.v; i < 4 * q.v + 4; i += 2) {
      e[i] = __builtin_amdgcn_exp2f(__builtin_fmaf(p[i], c, nm));
      e[i + 1] = __builtin_amdgcn_exp2f(__builtin_fmaf(p[i + 1], c, nm));
      s0 = i ? (s0 + e[i]) + e[i + 1] : e[i] + e[i + 1];
    }
    OPQ(s0);
  });
  const float rs = __builtin_amdgcn_rcpf(s0);
#pragma unroll
  for (int i = 0; i < 4; ++i) { o0[i] = pk(mul1(e[2 * i], rs), mul1(e[2 * i + 1], rs)); o1[i] = pk(mul1(e[8 + 2 * i], rs), mul1(e[9 + 2 * i], rs)); }
  asm volatile("" : "+v"(o0), "+v"(o1));
}
template <class H> DEV void gelu16(const f16v& a, float c0, float c1, float sig, v4u& o0, v4u& o1, H&& hook) {
  sfor<0, 4>([&](auto q) {
    hook(q);
    float g[4];
#pragma unroll
    for (int j = 0; j < 4; ++j) {
      const float v = a[4 * q.v + j];
      const float e = __builtin_amdgcn_exp2f(v * __builtin_fmaf(v * v, c1, c0));
      g[j] = mul1(v, __builtin_amdgcn_rcpf(__builtin_fmaf(e, sig, sig)));
    }
    v4u& o = q.v < 2 ? o0 : o1;
    u32 pa = pk(g[0], g[1]), pb = pk(g[2], g[3]);
    asm volatile("" : "+v"(pa), "+v"(pb));
    o[(q.v & 1) * 2] = pa; o[(q.v & 1) * 2 + 1] = pb;
  });
}
DEV void wait_flags(__amdgpu_buffer_rsrc_t rs, u32 voff, u32 tag, u32 tag2) {
#pragma unroll 1
  for (int sp = 0; sp < SPIN_CAP; ++sp) {
    const v2u f = __builtin_amdgcn_raw_buffer_load_b64(rs, voff, 0, 16);
    if (__all(f.x == tag && f.y == tag2)) break;
    asm volatile("s_sleep 2" ::: "memory");
  }
}
DEV void lds_arrive(u32 a) { asm volatile("s_waitcnt lgkmcnt(0)" ::: "memory"); *(volatile LAS u32*)a = 1u; }
DEV void lds_wait(u32 a) {
#pragma unroll 1
  for (int sp = 0; sp < 400000; ++sp) {
    const v4u f = *(volatile LAS v4u*)a;
    if (__builtin_amdgcn_readfirstlane(f.x & f.y & f.z & f.w)) break;
    asm volatile("s_sleep 1" ::: "memory");
  }
  asm volatile("" ::: "memory");
}

__global__ void __launch_bounds__(512, 2) hh_fused(Params p) {
  const u32 tid = threadIdx.x, lane = tid & 63u;
  const u32 w = __builtin_amdgcn_readfirstlane(tid >> 6);
  u32 b = blockIdx.x;
  u64 px0, pw0, pws0, px, pw, pws;
  asm volatile("s_load_dwordx2 %0, %4, 0x0\n\ts_load_dwordx2 %1, %4, %5\n\ts_load_dwordx2 %2, %4, 0x48" : "=&s"(px0), "=&s"(pw0), "=&s"(pws0), "+s"(b)
               : "s"(__builtin_amdgcn_kernarg_segment_ptr()), "s"(8u + ((b >> 3) & 8u)));
  u32 pfd;
  {
    u64 pc; u32 len;
    asm volatile("s_getpc_b64 %0\n.Lhh_pc:\n\ts_mov_b32 %1, .Lhh_end-.Lhh_pc" : "=s"(pc), "=s"(len));
    const u32 vo = (lane >> 1) * 65536u + (lane & 1u) * 128u;
    asm volatile("buffer_load_dword %0, %1, %2, %3 offen" : "=v"(pfd) : "v"(vo), "s"(mk_srd((const void*)pc, len)), "s"(((b & ~7u) + w) * 256u) : "memory");
    asm volatile("" : "+s"(b));
  }
  const u32 xcd = b & 7u, rb = (b >> 3) & 31u, xg = xcd >> 1, gl = rb >> 3;
  const u32 isM = gl & 1u;
  const u32 idx = xg * 2u + (gl >> 1);
  const u32 t = (xcd & 1u) * 8u + (rb & 7u);
  const u32 koff = ((rb & 7u) + 2u * gl) & 7u;
  const u32 r = w & 3u, hf = w >> 2;
  const u32 hh = lane >> 5, l31 = lane & 31u, q4 = lane & 3u, rr = (lane >> 2) & 3u, g1 = (lane >> 4) & 1u;
  const u32 seg0 = 5u * idx + (idx < 3u ? idx : 3u);
  const u32 six = idx < 3u;
  const u32 whalf = isM ? 1376u : 1536u, wstr = 2u * whalf;
  const u32 row7 = (lane >> 2) & 7u;
  u32 vx0 = (16u * w + row7) * 1024u + hh * 64u + 16u * (q4 ^ (row7 >> 2));
  u32 vx1 = ((vx0 + 8192u) ^ 32u) - 1024u;
  const u32 colb = isM ? seg0 * 64u : 1024u + idx * 128u;
  u32 vw0 = (8u * w + rr) * wstr + colb + g1 * 64u + hh * (isM ? 128u : 1024u) + 16u * q4 + 2048u;
  u32 vw1 = vw0 + 4u * wstr - 1024u;
  const u32 w4k = w * 4096u;
  u32 kx = koff * 128u, kw = kx * whalf, xs = t * 131072u, wn = 512u * wstr + 4096u;
  const u32 srow = (idx * 2u + isM) * 8u + w;
  u32 vrow = srow * 1024u + lane * 16u, ep0, ep;
  asm volatile("" : "+v"(vx0), "+v"(vx1), "+v"(vw0), "+v"(vw1), "+v"(vrow));
  asm volatile("s_mov_b32 m0, %7\n\ts_waitcnt lgkmcnt(0)\n\ts_load_dword %3, %6, 0x0\n\ts_mov_b64 %0, %4\n\ts_mov_b64 %1, %5\n\ts_mov_b64 %2, %6"
               : "=&s"(px), "=&s"(pw), "=&s"(pws), "=&s"(ep0) : "s"(px0), "s"(pw0), "s"(pws0), "s"(w4k), "s"(xs), "s"(wn), "s"(kx), "s"(kw));
  const v4u srdX = mk_srd((const char*)px + xs, 131072u);
  v4u xrow;
  asm volatile("buffer_load_dwordx4 %0, %1, %2, 0 offen" : "=v"(xrow) : "v"(vrow), "s"(srdX) : "memory");
  const v4u srdW = mk_srd((const char*)pw - 4096, wn);
  u32 vu = (8u * w + 4u * hh + rr) * wstr + (isM ? seg0 * 64u + 256u + (six ? g1 * 64u : 0u) : idx * 128u + g1 * 64u) + 16u * q4 + 4096u;
  auto dq = [&](auto qc) {
    constexpr int P = qc.v >> 2, e = qc.v & 3;
    if constexpr (P < 8) {
      if constexpr (e == 0) {
        if constexpr (P > 0) dma_nx<SLOT(P & 3)>(srdX, vx0, kx, kw, whalf, w4k);
        else dma<0>(srdX, vx0, kx);
      } else if constexpr (e == 1) dma<2048>(srdW, vw0, kw);
      else if constexpr (e == 2) dma<1024>(srdX, vx1, kx);
      else dma<3072>(srdW, vw1, kw);
    } else {
      dma_nw<SLOT(P & 3) + e * 1024>(srdW, vu, kx, kw, whalf, w4k);
    }
  };
  sfor<0, 4>(dq);
  const __attribute__((address_space(4))) Params* kp = (const __attribute__((address_space(4))) Params*)__builtin_amdgcn_kernarg_segment_ptr();
  asm volatile("s_barrier" : "+s"(kp) :: "memory");
  const u32 tb = (xcd & 1u) * 16u + xg * 4u + (rb & 3u), nb = rb >> 2;
  const __amdgpu_buffer_rsrc_t rsW = __builtin_amdgcn_make_buffer_rsrc((void*)pws, 0, WS_END, 0x00020000);
  asm volatile("s_waitcnt vmcnt(4) lgkmcnt(0)\n\ts_mov_b32 %0, %1" : "=s"(ep) : "s"(ep0) : "memory");
  asm volatile("" : "+v"(xrow), "+v"(pfd));
  const u32 tag = ep * 1664525u + 1013904223u, tag2 = (ep ^ 0x9e3779b9u) * 2654435761u + 12345u;
  {
    float xv[8];
#pragma unroll
    for (int i = 0; i < 4; ++i) { xv[2 * i] = blo(xrow[i]); xv[2 * i + 1] = bhi(xrow[i]); }
    const float mu = wave_sum(((xv[0] + xv[1]) + (xv[2] + xv[3])) + ((xv[4] + xv[5]) + (xv[6] + xv[7]))) * (1.f / 512.f);
    float q = 0.f;
#pragma unroll
    for (int i = 0; i < 8; ++i) q = __builtin_fmaf(xv[i] - mu, xv[i] - mu, q);
    const float var = wave_sum(q) * (1.f / 512.f);
    const float sg = __builtin_amdgcn_sqrtf(lane ? __builtin_fmaf(1e-5f, var + 1e-5f, var) : var + 1e-5f);
    __builtin_amdgcn_raw_buffer_store_b128((v4u){split2(-mu), tag, __float_as_uint(sg), tag2}, rsW,
                                           lane < 2u ? WS_ST + lane * 32768u + (t * 128u + srow) * 16u : 0x80000000u, 0, 16);
  }
  if (w >= 5u) {
    if (w == 7u) dma4(mk_srd(kp->b2, 1024u), nb * 128u + lane * 4u, SM_B2);
    else dma16(mk_srd(isM ? kp->b1 : kp->wphi, isM ? 2752u : 16384u), (isM ? seg0 * 64u : idx * 2048u + (w - 5u) * 1024u) + lane * 16u, SM_WB + (w - 5u) * 1024u);
  }
  sfor<4, 8>(dq);
  if (tid < 16u) *(LAS u32*)(SM_SYNC + tid * 4u) = 0u;
  v4u srdW2 = mk_srd(kp->w2, 1376u * 1024u), srdWo = mk_srd(kp->wo, 512u * 1024u);
  char* pout = kp->out;
  asm volatile("" : "+s"(srdW2), "+s"(srdWo), "+s"(pout));
  OPQ(vu);
  u32 sma = opq(SM_WB + (isM ? hf * 64u + l31 * 2u : hf * 1024u + (4u * hh + rr) * 32u + (2u * g1 + (q4 >> 1)) * 8u));
  u32 vp = opq((4u * hh + rr) * 1024u + nb * 128u + g1 * 64u + 16u * q4);
  u32 csr = opq(hh ? SM_ZERO : SM_CS + (hf * 32u + l31) * 48u), csw = opq(csr + r * 4u);
  u32 vst = opq(WS_ST + isM * 32768u + (t * 128u + r * 32u + l31) * 16u);
  const u32 mrow = w & 1u, kh = (w >> 1) & 1u;
  u32 pa0 = opq(hh * 512u + rr * 64u + g1 * 32u + q4 * 8u + (hf ? SLOT(2) + kh * 32768u : kh * 10240u));
  const u32 tok32 = 4u * t + r;
  const u32 un = (r >> 1) * 4u + 2u * hf + (r & 1u);
  const u32 pi = (idx * 8u + un) * 16u + t;
  u32 ad[7];
  {
    const u32 sw = (l31 >> 2) & 1u, tk32 = (32u * r + l31) * 32u;
    const u32 la = (16u * hh + 4u * g1 + rr) * 32u + ((q4 * 8u) ^ (g1 * 16u));
    ad[0] = opq(isM ? WS_H + (tok32 * 86u + seg0 * 2u + hf * 2u) * 1024u + lane * 16u : WS_KVP + pi * 512u + lane * 8u);
    ad[1] = opq((lane & 15u) == 0u ? WS_ZP + pi * 64u + (lane >> 4) * 16u : 0x80000000u);
    ad[2] = opq(IM_PHI + hh * 4096u + tk32 + sw * 16u);
    ad[3] = opq(ad[2] ^ 16u);
    ad[4] = opq(IM_V + tk32 + (hh ^ sw) * 16u);
    ad[5] = opq(IM_PHI + ((r & 1u) * 2u + (r >> 1)) * 4096u + la);
    ad[6] = opq(IM_V + (r & 1u) * 4096u + la);
  }

  const u32 rot = r ^ (2u * hf);
  const u32 xa0 = (2u * r + g1) * 4096u + ((l31 >> 3) & 1u) * 1024u + (l31 & 7u) * 64u + ((16u * (hh ^ ((l31 >> 2) & 3u))) ^ ((rot & 1u) * 32u)) + (rot >> 1) * 512u;
  const u32 wl = hf * 256u + rr * 64u + g1 * 32u + q4 * 8u;
  const u32 wa0 = rot * 8192u + hh * 4096u + wl;
  u32 xa[4], wa[4];
#pragma unroll
  for (u32 s = 0; s < 4; ++s) { xa[s] = xa0 ^ ((s >> 1) * 512u + (s & 1u) * 32u); wa[s] = wa0 ^ (s * 8192u); }
  const u32 one2 = 0x3f803f80u;
  const u32 selv = ((lane & 15u) == (g1 ? 4u : 0u)) ? one2 : 0u;
  v4u sel = {selv, selv, selv, selv};
  asm volatile("" : "+v"(xa[0]), "+v"(xa[1]), "+v"(xa[2]), "+v"(xa[3]), "+v"(wa[0]), "+v"(wa[1]), "+v"(wa[2]), "+v"(wa[3]), "+v"(sel));

  v4u xr[32];
  v2u wf[16];
  f16v acc0, acc1;
  f4 cs0 = {0.f, 0.f, 0.f, 0.f}, cs1 = {0.f, 0.f, 0.f, 0.f};
#pragma unroll
  for (int i = 0; i < 16; ++i) { acc0[i] = 0.f; acc1[i] = 0.f; }
  v4u aphi[2], rec, fx;
  float rinv, sig;
  auto frag = [&](int s, int u) { const v2u a = wf[s * 4 + u * 2], c = wf[s * 4 + u * 2 + 1]; return (v4u){a.x, a.y, c.x, c.y}; };
  sfor<0, 8>([&](auto cpc) {
    constexpr int cp = cpc.v;
    constexpr u32 sl = SLOT(cp & 1);
    auto rx = [&](int s) { xr[cp * 4 + s] = lds16(xa[s] + sl); };
    auto rw = [&](int s, int u, int h) { wf[s * 4 + u * 2 + h] = ldstr(wa[s] + sl + 2048u + u * 512u + h * 1024u); };
    if constexpr (cp == 5) { BAR_VM(5); } else { BAR_VM(4); }
    auto flip = [&](int s) {
      if constexpr (cp & 1) { wa[s] = opq(wa[s] ^ 65536u); if constexpr (cp < 7) xa[s] = opq(xa[s] ^ 65536u); }
    };
    FENCE();
    if constexpr (cp > 0) acc0 = mfma32(frag(2, 0), xr[cp * 4 - 2], acc0);
    FENCE();
    rx(0); rw(0, 0, 0); rw(0, 0, 1);
    FENCE();
    if constexpr (cp > 0) acc1 = mfma32(frag(2, 1), xr[cp * 4 - 2], acc1);
    FENCE();
    rw(0, 1, 0); rw(0, 1, 1); rx(1);
    if constexpr (cp > 0) { FENCE(); dq(IC<cp * 4 + 8>{}); }
    FENCE();
    if constexpr (cp > 0) acc0 = mfma32(frag(3, 0), xr[cp * 4 - 1], acc0);
    FENCE();
    rw(1, 0, 0); rw(1, 0, 1); rw(1, 1, 0);
    flip(0);
    FENCE();
    if constexpr (cp > 0) acc1 = mfma32(frag(3, 1), xr[cp * 4 - 1], acc1);
    FENCE();
    rw(1, 1, 1); rw(2, 0, 0); rw(2, 0, 1); rx(2);
    if constexpr (cp > 0) { FENCE(); dq(IC<cp * 4 + 9>{}); }
    FENCE();
    if constexpr (cp == 7) { cs0 = mfma16(sel, frag(0, 0), cs0); cs1 = mfma16(sel, frag(0, 1), cs1); }
    acc0 = mfma32(frag(0, 0), xr[cp * 4], acc0);
    FENCE();
    rw(2, 1, 0); rw(2, 1, 1); rx(3);
    flip(1);
    if constexpr (cp == 6) {
      OPQ(rec);
      if (!__all(rec[1] == tag && rec[3] == tag2)) {
#pragma unroll 1
        for (int sp = 0; sp < SPIN_CAP; ++sp) {
          rec = __builtin_amdgcn_raw_buffer_load_b128(rsW, vst, 0, 16);
          if (__all(rec[1] == tag && rec[3] == tag2)) break;
          asm volatile("s_sleep 2" ::: "memory");
        }
      }
    }
    FENCE();
    acc1 = mfma32(frag(0, 1), xr[cp * 4], acc1);
    FENCE();
    rw(3, 0, 0); rw(3, 0, 1); rw(3, 1, 0); rw(3, 1, 1);
    if constexpr (cp > 0) { FENCE(); dq(IC<cp * 4 + 10>{}); }
    FENCE();
    if constexpr (cp < 7) { cs0 = mfma16(sel, frag(0, 0), cs0); cs1 = mfma16(sel, frag(0, 1), cs1); }
    FENCE();
    flip(2);
    if constexpr (cp == 6) { sig = __uint_as_float(rec[2]); rinv = __builtin_amdgcn_rcpf(sig); OPQ(rinv); }
    if constexpr (cp == 7) { *(LAS float*)(csw) = cs0[0]; *(LAS float*)(csw + 16u) = cs1[0]; }
    FENCE();
    acc0 = mfma32(frag(1, 0), xr[cp * 4 + 1], acc0);
    FENCE();
    flip(3);
    if constexpr (cp == 6) { fx = (v4u){rec[0], rec[0], split2(sig), 0u}; OPQ(fx); }
    if constexpr (cp > 0) { FENCE(); dq(IC<cp * 4 + 11>{}); }
    if constexpr (cp == 4) ld16_sc1(rec, mk_srd((const void*)pws, WS_END), vst);
    FENCE();
    acc1 = mfma32(frag(1, 1), xr[cp * 4 + 1], acc1);
    if constexpr (cp == 0) {
      FENCE();
      if (isM) {
        const u32 bA = *(LAS unsigned short*)sma, bB = *(LAS unsigned short*)(sma + 128u), bC = *(LAS unsigned short*)(sma + (six ? 256u : 192u));
        const u32 m = hh ? 0u : 0x10001u;
        aphi[0] = (v4u){bA * m, bB * m, bC * m, 0u}; aphi[1] = (v4u){0u, 0u, 0u, 0u};
      } else {
        const u32 sgn = (lane & 4u) ? 0x80008000u : 0u;
#pragma unroll
        for (u32 e = 0; e < 2; ++e) {
          aphi[e] = ldsfrag(sma + e * 512u, 256u);
          aphi[e] ^= (v4u){sgn, sgn, sgn, sgn};
        }
      }
      OPQ(aphi[0]); OPQ(aphi[1]);
      FENCE();
      sfor<8, 12>(dq);
    }
    FENCE();
  });
  BAR_VM(4);
  u32 gs;
  auto wdma = [&](auto ec, v4u srd, u32 lds, u32 mem) {
    FENCE();
    dma_grp<ec.v>(srd, vp, gs, lds, mem);
    FENCE();
  };
  {
    FENCE();
    acc0 = mfma32(frag(2, 0), xr[30], acc0);
    FENCE();
    const f4 ka = *(LAS f4*)(csr), kb = *(LAS f4*)(csr + 16u);
    wdma(IC<0>{}, srdWo, SLOT(2) + w4k, w4k * 8u);
    acc0 = mfma32(frag(3, 0), xr[31], acc0);
    wdma(IC<1>{}, srdWo, 0u, 0u);
    acc1 = mfma32(frag(2, 1), xr[30], acc1);
    FENCE();
    v4u fa = fold_w(ka, isM ? aphi[0][0] : 0u);
    OPQ(fa);
    wdma(IC<2>{}, srdWo, 0u, 0u);
    acc0 = mfma32(fa, fx, acc0);
    FENCE();
    v4u fb = fold_w(kb, isM ? aphi[0][1] : 0u);
    OPQ(fb);
    FENCE();
    acc1 = mfma32(frag(3, 1), xr[31], acc1);
    wdma(IC<3>{}, srdWo, 0u, 0u);
    acc1 = mfma32(fb, fx, acc1);
    FENCE();
  }
  u32 tidv = tid;
  OPQ(tidv);
  auto tail = [&](auto ismc, auto hfc) {
  constexpr bool ISM = decltype(ismc)::v; constexpr u32 hf = decltype(hfc)::v;
  const u32 tid = tidv, lane = tid & 63u;
  const u32 hh = lane >> 5, l31 = lane & 31u, q4 = lane & 3u, rr = (lane >> 2) & 3u, g1 = (lane >> 4) & 1u;
  auto nohook = [&](auto) {};
  auto wgrp = [&](v4u srd, u32 lds, u32 mem) { sfor<0, 4>([&](auto e) { wdma(e, srd, lds, mem); }); };
  auto w2grp = [&](u32 g) { wgrp(srdW2, g * 4096u, g * 32768u); };
  auto hk_wo1 = [&](auto e) { wdma(e, srdWo, SLOT(3) + w4k, w4k * 8u + 262144u); };
  auto hk_s0 = [&](auto e) { wdma(e, srdW2, w4k, w4k * 8u + (w < 5u ? 1216u * 1024u : 0u)); };
  auto hk_s2 = [&](auto e) { wdma(e, srdW2, SLOT(2) + w4k, (SLOT(2) + w4k) * 8u); };

  auto put_flag = [&](u32 off) {
    __builtin_amdgcn_raw_buffer_store_b64((v2u){tag, tag2}, rsW, lane == 0u ? off : 0x80000000u, 0, 16);
  };
  f16v acc2;
  f4 cs2 = {0.f, 0.f, 0.f, 0.f};
#pragma unroll
  for (int i = 0; i < 16; ++i) acc2[i] = 0.f;
  auto g2 = [&](auto i0c, auto i1c, auto&& hook) {
    constexpr int i0 = i0c.v, i1 = i1c.v;
    v4u u[i1 - i0];
    auto rd = [&](int i) { u[i - i0] = ldsfrag(wa[i & 3] + SLOT(i >> 4) + ((i >> 2) & 3) * 1024u, 512u); };
    rd(i0); rd(i0 + 1);
    sfor<i0, i1>([&](auto ic) {
      constexpr int i = ic.v;
      if constexpr (i + 2 < i1) rd(i + 2);
      FENCE();
      acc2 = mfma32(u[i - i0], xr[i], acc2);
      FENCE();
      if constexpr ((i & 3) == 0) cs2 = mfma16(sel, u[i - i0], cs2);
      hook(IC<i - i0>{});
    });
  };
  auto put_cs2 = [&]() { *(LAS float*)(csw + 32u) = cs2[0]; };
  auto fold2 = [&](u32 bb) { acc2 = mfma32(fold_w(*(LAS f4*)(csr + 32u), bb), fx, acc2); };

  v4u wr[32];
  auto to_regs = [&]() {
#pragma unroll
    for (u32 i = 0; i < (hf ? 16 : 5); ++i) { wr[2 * i] = ldsfrag(pa0 + i * 2048u, 1024u); wr[2 * i + 1] = ldsfrag(pa0 + i * 2048u + 256u, 1024u); }
  };
  auto st16 = [&](v4u v, u32 voff, u32 soff = 0u) { __builtin_amdgcn_raw_buffer_store_b128(v, rsW, voff, soff, 16); };
  auto st32 = [&](v4u a, v4u c, u32 voff, u32 soff) { st16(a, voff, soff); st16(c, voff + 1024u, soff); hold(a, c); };

  if constexpr (ISM) {
    const float c0 = -2.302208198f * rinv;
    const float c1 = -0.1029432397f * rinv * rinv * rinv;
    const u32 hb = ad[0], bC = aphi[0][2];
    v4u o0, o1;
    if constexpr (!hf) {
      gelu16(acc0, c0, c1, sig, o0, o1, hk_wo1);
      st32(o0, o1, hb, 0u);
      BAR_VM(10);
      gelu16(acc1, c0, c1, sig, o0, o1, nohook);
      st32(o0, o1, hb, 4096u);
      if (six) g2(IC<0>{}, IC<16>{}, nohook);
      BAR_VM(4);
      if (six) g2(IC<16>{}, IC<24>{}, nohook);
      BAR();
      if (six) g2(IC<24>{}, IC<32>{}, [&](auto i) { if constexpr (i.v < 4) hk_s0(i); else hk_s2(IC<i.v - 4>{}); });
      else { sfor<0, 4>(hk_s0); sfor<0, 4>(hk_s2); }
      put_cs2();
      BAR_VM(4);
      to_regs();
      BAR();
      if (six) {
        fold2(bC);
        gelu16(acc2, c0, c1, sig, o0, o1, [&](auto e) { wdma(e, srdW2, (8u + r) * 4096u, (8u + r) * 32768u); });
        st32(o0, o1, hb, 8192u);
      } else w2grp(8u + r);
      w2grp(12u + r); w2grp(r);
      WAIT_VM(8);
    } else {
      g2(IC<0>{}, IC<16>{}, [&](auto i) { if constexpr ((i.v & 3) == 1) hk_wo1(IC<(i.v >> 2)>{}); });
      BAR_VM(8);
      g2(IC<16>{}, IC<32>{}, nohook);
      put_cs2();
      gelu16(acc0, c0, c1, sig, o0, o1, nohook);
      st32(o0, o1, hb, 0u);
      BAR_VM(2);
      to_regs();
      BAR();
      gelu16(acc1, c0, c1, sig, o0, o1, [&](auto q) { if constexpr (q.v < 2) { hk_s0(IC<2 * q.v>{}); hk_s0(IC<2 * q.v + 1>{}); } else { hk_s2(IC<2 * q.v - 4>{}); hk_s2(IC<2 * q.v - 3>{}); } });
      st32(o0, o1, hb, 4096u);
      BAR_VM(6);
      fold2(bC);
      BAR();
      gelu16(acc2, c0, c1, sig, o0, o1, [&](auto e) { wdma(e, srdW2, (24u + r) * 4096u, (24u + r) * 32768u); });
      st32(o0, o1, hb, six ? 8192u : 6144u);
      w2grp(28u + r); w2grp(32u + r);
      WAIT_VM(8);
    }
    put_flag(WS_FH + (tok32 * 16u + idx * 2u + hf) * FSTR);
    if constexpr (hf) { if (r < 3u) w2grp(r == 0u ? 4u : 35u + r); }
  } else {
    const float cl = rinv * 1.4426950409f;
    f16v zero;
#pragma unroll
    for (int i = 0; i < 16; ++i) zero[i] = 0.f;
    v4u o0, o1, vv[2];
    f16v pp[2];
    auto logits = [&](const f16v& a) {
      const v4u k0 = pk8(a, 0), k1 = pk8(a, 8);
      pp[0] = mfma32(aphi[0], k0, zero); pp[1] = mfma32(aphi[1], k1, zero);
      FENCE();
    };
    auto put_phi = [&](u32 e) { *(LAS v4u*)(ad[2] + e * 8192u) = o0; *(LAS v4u*)(ad[3] + e * 8192u) = o1; };
    auto scale_v = [&]() {
#pragma unroll
      for (int i = 0; i < 8; ++i) vv[i >> 2][i & 3] = pk(mul1(acc1[2 * i], rinv), mul1(acc1[2 * i + 1], rinv));
      asm volatile("" : "+v"(vv[0]), "+v"(vv[1]));
      FENCE();
    };
    auto put_v = [&]() { *(LAS v4u*)(ad[4]) = vv[0]; *(LAS v4u*)(ad[4] + 4096u) = vv[1]; };
    v4u fa[4], fb[4];
    auto kv_read = [&]() {
#pragma unroll
      for (u32 ks = 0; ks < 4; ++ks) { fa[ks] = ldsfrag(ad[5] + ks * 1024u, 256u); fb[ks] = ldsfrag(ad[6] + ks * 1024u, 256u); }
    };
    auto kv_put = [&]() {
      f4 akv = {0.f, 0.f, 0.f, 0.f}, az = {0.f, 0.f, 0.f, 0.f};
      const v4u ones = {one2, one2, one2, one2};
#pragma unroll
      for (u32 ks = 0; ks < 4; ++ks) { akv = mfma16(fa[ks], fb[ks], akv); az = mfma16(fa[ks], ones, az); }
      __builtin_amdgcn_raw_buffer_store_b64((v2u){pk(akv[0], akv[1]), pk(akv[2], akv[3])}, rsW, ad[0], 0, 16);
      st16(__builtin_bit_cast(v4u, az), ad[1]);
    };
    v4u pv[8]; f4 z;
    auto gather_ld = [&]() {
      wait_flags(rsW, WS_FKV + ((idx * 8u + un) * 16u + (lane & 15u)) * FSTR, tag, tag2);
#pragma unroll
      for (u32 i = 0; i < 8; ++i) pv[i] = __builtin_amdgcn_raw_buffer_load_b128(rsW, lane * 16u + (i & 3u) * 1024u, WS_KVP + (idx * 8u + un) * 8192u + (i >> 2) * 4096u, 16);
      z = __builtin_bit_cast(f4, __builtin_amdgcn_raw_buffer_load_b128(rsW, lane * 16u, WS_ZP + (idx * 8u + un) * 1024u, 16));
      FENCE();
    };
    auto gather_sum = [&]() {
      float s8[8];
#pragma unroll
      for (int j = 0; j < 4; ++j) { s8[2 * j] = blo(pv[0][j]); s8[2 * j + 1] = bhi(pv[0][j]); }
#pragma unroll
      for (int i = 1; i < 8; ++i)
#pragma unroll
        for (int j = 0; j < 4; ++j) { s8[2 * j] += blo(pv[i][j]); s8[2 * j + 1] += bhi(pv[i][j]); }
      float tt[4];
#pragma unroll
      for (int j = 0; j < 4; ++j) {
        const v2u sw2 = __builtin_amdgcn_permlane32_swap(__float_as_uint(s8[j]), __float_as_uint(s8[4 + j]), false, false);
        tt[j] = __uint_as_float(sw2.x) + __uint_as_float(sw2.y);
      }
      const u32 c = ((2u * l31) & 15u) + hh, d = (c & 3u) + 8u * ((c >> 2) & 1u) + 4u * (c >> 3);
      *(LAS v2u*)(SM_KVT + un * 544u + d * 32u + (l31 >> 3) * 8u) = (v2u){pk(tt[0], tt[1]), pk(tt[2], tt[3])};
#pragma unroll
      for (int j = 0; j < 4; ++j) {
        float v = z[j];
        v += __uint_as_float(__builtin_amdgcn_update_dpp(0u, __float_as_uint(v), 0x124, 0xf, 0xf, false));
        v += __uint_as_float(__builtin_amdgcn_update_dpp(0u, __float_as_uint(v), 0x128, 0xf, 0xf, false));
        v2u s2 = __builtin_amdgcn_permlane16_swap(__float_as_uint(v), __float_as_uint(v), false, false);
        v = __uint_as_float(s2.x) + __uint_as_float(s2.y);
        s2 = __builtin_amdgcn_permlane32_swap(__float_as_uint(v), __float_as_uint(v), false, false);
        z[j] = __uint_as_float(s2.x) + __uint_as_float(s2.y);
      }
      if (lane < 4u) *(LAS v2u*)(SM_KVT + un * 544u + 512u + lane * 8u) = (v2u){pk(z[0], z[1]), pk(z[2], z[3])};
    };
    v4u phq[4];
    const u32 ka = opq(SM_KVT + (2u * hf + 4u * hh) * 544u + (l31 < 16u ? l31 : 16u) * 32u);
    auto put_o = [&]() {
      f16v d[2]; v4u ov[2];
      const v4u a0 = lds16(ka), a1 = lds16(ka + 544u), a2 = lds16(ka + 16u), a3 = lds16(ka + 560u);
      d[0] = mfma32(a0, phq[0], zero); d[1] = mfma32(a1, phq[2], zero);
      d[0] = mfma32(a2, phq[1], d[0]); d[1] = mfma32(a3, phq[3], d[1]);
#pragma unroll
      for (u32 e = 0; e < 2; ++e) {
        const float rd = __builtin_amdgcn_rcpf(d[e][8] + 1e-6f);
#pragma unroll
        for (int i = 0; i < 4; ++i) ov[e][i] = pk(mul1(d[e][2 * i], rd), mul1(d[e][2 * i + 1], rd));
      }
      st32(ov[0], ov[1], lane * 16u, WS_O + (tok32 * 32u + idx * 4u + 2u * hf) * 1024u);
    };
    if constexpr (!hf) {
      logits(acc0); scale_v(); put_v();
      softmax16(pp[0], cl, o0, o1, hk_wo1); put_phi(0);
      BAR_VM(8);
      softmax16(pp[1], cl, o0, o1, nohook); put_phi(1);
      BAR();
      kv_read(); kv_put();
      g2(IC<0>{}, IC<16>{}, nohook);
      BAR_VM(2);
      g2(IC<16>{}, IC<24>{}, nohook);
      WAIT_VM(0);
      put_flag(WS_FKV + pi * FSTR);
      BAR();
      g2(IC<24>{}, IC<32>{}, [&](auto i) { if constexpr (i.v < 4) hk_s0(i); else hk_s2(IC<i.v - 4>{}); });
      put_cs2();
      BAR_VM(4);
      to_regs();
      fold2(0u);
      BAR();
      logits(acc2);
      softmax16(pp[0], cl, phq[0], phq[1], nohook);
      gather_ld();
      softmax16(pp[1], cl, phq[2], phq[3], [&](auto e) { wdma(e, srdW2, (8u + r) * 4096u, (8u + r) * 32768u); });
      gather_sum();
      BAR();
      put_o();
      w2grp(12u + r);
      WAIT_VM(4);
      put_flag(WS_FO + (tok32 * 16u + idx * 2u) * FSTR);
    } else {
      g2(IC<0>{}, IC<16>{}, [&](auto i) { if constexpr ((i.v & 3) == 1) hk_wo1(IC<(i.v >> 2)>{}); });
      BAR_VM(8);
      g2(IC<16>{}, IC<32>{}, nohook);
      put_cs2();
      BAR();
      logits(acc0); scale_v();
      softmax16(pp[0], cl, o0, o1, nohook);
      BAR_VM(0);
      to_regs();
      put_phi(0); put_v();
      BAR();
      softmax16(pp[1], cl, o0, o1, [&](auto q) { if constexpr (q.v < 2) { hk_s0(IC<2 * q.v>{}); hk_s0(IC<2 * q.v + 1>{}); } else { hk_s2(IC<2 * q.v - 4>{}); hk_s2(IC<2 * q.v - 3>{}); } });
      put_phi(1);
      BAR_VM(4);
      kv_read();
      BAR();
      kv_put();
      w2grp(24u + r); w2grp(28u + r);
      fold2(0u);
      WAIT_VM(8);
      put_flag(WS_FKV + pi * FSTR);
      logits(acc2);
      softmax16(pp[0], cl, phq[0], phq[1], [&](auto e) { wdma(e, srdW2, r * 4096u, r * 32768u); });
      softmax16(pp[1], cl, phq[2], phq[3], [&](auto e) { wdma(e, srdW2, (32u + r) * 4096u, (32u + r) * 32768u); });
      if (r < 3u) w2grp(r == 0u ? 4u : 35u + r);
      BAR();
      BAR_VM(0);
      gather_ld();
      gather_sum();
      lds_arrive(SM_SYNC + r * 4u);
      lds_wait(SM_SYNC);
      put_o();
      WAIT_VM(0);
      put_flag(WS_FO + (tok32 * 16u + idx * 2u + 1u) * FSTR);
    }
  }

  const u32 tk = 2u * tb + mrow;
  f16v d0, d1;
#pragma unroll
  for (int i = 0; i < 16; ++i) { d0[i] = 0.f; d1[i] = 0.f; }
  const u32 ra = opq((hf * 2u + kh) * 17408u + (32u * mrow + l31) * 272u + hh * 16u);
  auto put_part = [&]() {
#pragma unroll
    for (int g = 0; g < 4; ++g) {
      *(LAS f4*)(ra + g * 32u) = (f4){d0[4 * g], d0[4 * g + 1], d0[4 * g + 2], d0[4 * g + 3]};
      *(LAS f4*)(ra + 128u + g * 32u) = (f4){d1[4 * g], d1[4 * g + 1], d1[4 * g + 2], d1[4 * g + 3]};
    }
  };
  if constexpr (!hf) {
    constexpr int WIN = MWIN;
    const u32 hv = WS_H + (tk * 86u + kh * 38u) * 1024u;
    const u32 hs = (nb & 1u) * 19u;
    const u32 hvA = opq(hv + hs * 1024u + lane * 16u), hvB = opq(hv + (19u - hs) * 1024u + lane * 16u), hvT = opq(hv + (76u - kh * 33u) * 1024u + lane * 16u);
    const u32 laA = opq(pa0 + (kh * 33u + hs) * 2048u), laB = opq(pa0 + (kh * 33u + 19u - hs) * 2048u);
    const u32 xo = opq((64u * tb + (tid >> 3)) * 1024u + nb * 128u + (tid & 7u) * 16u);
    const u32 fa = opq((tid >> 3) * 272u + (tid & 7u) * 32u);
    BAR_VM(1);
    wait_flags(rsW, WS_FH + (tk * 16u + (lane & 15u)) * FSTR, tag, tag2);
    auto ldact = [&](int i) {
      const int j = i < 5 ? i : i < 24 ? i - 5 : i - 24;
      return __builtin_amdgcn_raw_buffer_load_b128(rsW, (i < 5 ? hvT : i < 24 ? hvA : hvB) + (j & 3) * 1024, (j >> 2) * 4096, 16);
    };
    const __amdgpu_buffer_rsrc_t rsX = __builtin_amdgcn_make_buffer_rsrc((void*)px, 0, 2097152, 0x00020000);
    v4u act[43], xres[2];
#pragma unroll
    for (int i = 0; i < WIN; ++i) act[i] = ldact(i);
    FENCE();
    v4u n0, n1;
#pragma unroll
    for (int i = 0; i < 43; ++i) {
      v4u c0, c1;
      if (i < 5) { c0 = wr[2 * i]; c1 = wr[2 * i + 1]; } else { c0 = n0; c1 = n1; }
      d0 = mfma32(c0, act[i], d0);
      FENCE();
      if (i + WIN < 43) act[i + WIN] = ldact(i + WIN);
      else if (i + WIN < 45) xres[i + WIN - 43] = __builtin_amdgcn_raw_buffer_load_b128(rsX, xo, (i + WIN - 43) * 32768, 0);
      if (i + 1 >= 5 && i + 1 < 43) { const u32 a = i + 1 < 24 ? laA + (i - 4) * 2048u : laB + (i - 23) * 2048u; n0 = ldsfrag(a, 1024u); n1 = ldsfrag(a + 256u, 1024u); }
      FENCE();
      d1 = mfma32(c1, act[i], d1);
      FENCE();
    }
    lds_arrive(SM_SYNC + 16u + r * 4u);
    const v4u bb = lds16(SM_B2 + (tid & 7u) * 16u);
    lds_wait(SM_SYNC + 16u);
    put_part();
    lds_arrive(SM_SYNC + 32u + r * 4u);
    lds_wait(SM_SYNC + 32u);
    float gt[16], xf[16];
#pragma unroll
    for (int q = 0; q < 4; ++q) {
      const u32 a = fa + (q >> 1) * 8704u + (q & 1) * 16u;
      const f4 m0 = *(LAS f4*)(a), m1 = *(LAS f4*)(a + 17408u);
#pragma unroll
      for (int j = 0; j < 4; ++j) {
        const u32 bw = bb[(q & 1) * 2 + (j >> 1)], xw = xres[q >> 1][(q & 1) * 2 + (j >> 1)];
        const float m = (m0[j] + m1[j]) + ((j & 1) ? bhi(bw) : blo(bw));
        gt[q * 4 + j] = m * __builtin_amdgcn_rcpf(1.f + __builtin_amdgcn_exp2f(m * -1.4426950409f));
        xf[q * 4 + j] = (j & 1) ? bhi(xw) : blo(xw);
        asm volatile("" : "+v"(gt[q * 4 + j]), "+v"(xf[q * 4 + j]));
      }
    }
    BAR();
    const __amdgpu_buffer_rsrc_t rsO = __builtin_amdgcn_make_buffer_rsrc(pout, 0, 2097152, 0x00020000);
#pragma unroll
    for (int e = 0; e < 2; ++e) {
      float res[8];
#pragma unroll
      for (int hx = 0; hx < 2; ++hx) {
        const u32 a = fa + 34816u + e * 8704u + hx * 16u;
        const f4 s0 = *(LAS f4*)(a), s1 = *(LAS f4*)(a + 17408u);
#pragma unroll
        for (int j = 0; j < 4; ++j) res[hx * 4 + j] = __builtin_fmaf(s0[j] + s1[j], gt[e * 8 + hx * 4 + j], xf[e * 8 + hx * 4 + j]);
      }
      const v4u ov = {pk(res[0], res[1]), pk(res[2], res[3]), pk(res[4], res[5]), pk(res[6], res[7])};
      __builtin_amdgcn_raw_buffer_store_b128(ov, rsO, xo, e * 32768, 17);
      hold(ov, ov);
    }
  } else {
    if constexpr (ISM) { BAR_VM(0); }
    if (b == 0u && w == 7u) {
      wait_flags(rsW, WS_FH + ((lane >> 3) * 64u + (lane & 7u) * 2u) * FSTR, tag, tag2);
      wait_flags(rsW, WS_FH + ((lane >> 3) * 64u + 512u + (lane & 7u) * 2u) * FSTR, tag, tag2);
    }
    const u32 ov = WS_O + (tk * 32u + kh * 16u) * 1024u;
#pragma unroll
    for (u32 ph = 0; ph < 2; ++ph) {
      wait_flags(rsW, WS_FO + (tk * 16u + kh * 8u + (lane & 3u) * 2u + ph) * FSTR, tag, tag2);
      v4u act[8];
#pragma unroll
      for (u32 i = 0; i < 8; ++i) act[i] = __builtin_amdgcn_raw_buffer_load_b128(rsW, lane * 16u + (ph * 2u + (i & 1u)) * 1024u, ov + (i >> 1) * 4096u, 16);
      FENCE();
      if (ph) lds_wait(SM_SYNC + 16u);
#pragma unroll
      for (u32 i = 0; i < 8; ++i) { const u32 k = (i >> 1) * 4u + ph * 2u + (i & 1u); d0 = mfma32(wr[2 * k], act[i], d0); d1 = mfma32(wr[2 * k + 1], act[i], d1); }
    }
    put_part();
    BAR();
    if (b == 0u && w == 7u) __builtin_amdgcn_raw_buffer_store_b32(tag, rsW, lane == 0u ? WS_E : 0x80000000u, 0, 16);
  }
  };
  auto finish = [&]() {
    asm volatile("s_endpgm" : "=v"(xr[0]), "=v"(xr[1]), "=v"(xr[2]), "=v"(xr[3]), "=v"(xr[4]), "=v"(xr[5]), "=v"(xr[6]), "=v"(xr[7]),
                 "=v"(xr[8]), "=v"(xr[9]), "=v"(xr[10]), "=v"(xr[11]), "=v"(xr[12]), "=v"(xr[13]), "=v"(xr[14]), "=v"(xr[15]), "=v"(acc0), "=v"(acc1),
                 "=v"(fx), "=v"(rinv), "=v"(sig), "=v"(vp), "=v"(aphi[0]), "=v"(aphi[1]), "=v"(sel), "=v"(csr), "=v"(csw), "=v"(pa0) :: "memory");
    asm volatile("" : "=v"(xr[16]), "=v"(xr[17]), "=v"(xr[18]), "=v"(xr[19]), "=v"(xr[20]), "=v"(xr[21]), "=v"(xr[22]), "=v"(xr[23]),
                 "=v"(xr[24]), "=v"(xr[25]), "=v"(xr[26]), "=v"(xr[27]), "=v"(xr[28]), "=v"(xr[29]), "=v"(xr[30]), "=v"(xr[31]), "=v"(tidv),
                 "=v"(wa[0]), "=v"(wa[1]), "=v"(wa[2]), "=v"(wa[3]), "=v"(ad[0]), "=v"(ad[1]), "=v"(ad[2]), "=v"(ad[3]), "=v"(ad[4]), "=v"(ad[5]), "=v"(ad[6]) :: "memory");
  };
  u32 role = isM * 2u + hf;
  if (role == 0u) { tail(IC<0>{}, IC<0>{}); finish(); }
  asm volatile("" : "+s"(role));
  if (role == 1u) { tail(IC<0>{}, IC<1>{}); finish(); }
  asm volatile("" : "+s"(role));
  if (role == 2u) { tail(IC<1>{}, IC<0>{}); finish(); }
  tail(IC<1>{}, IC<1>{});
  asm volatile(".Lhh_end:");
}

extern "C" void kernel_launch(void* const* d_in, const int* in_sizes, int n_in,
                              void* d_out, int out_size, void* d_ws, size_t ws_size,
                              hipStream_t stream) {
  Params p;
  p.x = (const char*)d_in[0]; p.wqkv = (const char*)d_in[1]; p.wphi = (const char*)d_in[2]; p.wo = (const char*)d_in[3];
  p.w1 = (const char*)d_in[5]; p.b1 = (const char*)d_in[6]; p.w2 = (const char*)d_in[7]; p.b2 = (const char*)d_in[8];
  p.out = (char*)d_out; p.ws = (char*)d_ws;
  (void)hipFuncSetAttribute((const void*)hh_fused, hipFuncAttributeMaxDynamicSharedMemorySize, (int)LDS_BYTES);
  hh_fused<<<256, 512, LDS_BYTES, stream>>>(p);
}
```

```cpp
#include <hip/hip_runtime.h>
#pragma clang diagnostic ignored "-Wint-to-pointer-cast"

typedef unsigned u32;
typedef unsigned long long u64;
typedef u32 v4u __attribute__((ext_vector_type(4)));
typedef u32 v2u __attribute__((ext_vector_type(2)));
typedef float f2 __attribute__((ext_vector_type(2)));
typedef float f4 __attribute__((ext_vector_type(4)));
typedef float f16v __attribute__((ext_vector_type(16)));
typedef __bf16 bfx2 __attribute__((ext_vector_type(2)));
typedef __bf16 bfx8 __attribute__((ext_vector_type(8)));
typedef short s4 __attribute__((ext_vector_type(4)));
#define LAS __attribute__((address_space(3)))
#define DEV __device__ __forceinline__

#define FSTR 128u
#define WS_E 0u
#define WS_FC 4096u
#define WS_FH 8192u
#define WS_FL (WS_FH + 1024u * FSTR)
#define WS_ZP (WS_FL + 1024u * FSTR)
#define WS_KVP (WS_ZP + 32768u)
#define WS_O (WS_KVP + 524288u)
#define WS_H (WS_O + 2097152u)
#define WS_ST (WS_H + 64u * 86u * 1024u)
#define WS_END (WS_ST + 65536u)
#define SLOT(i) ((u32)(i) * 32768u)
#define IM_PHI 131072u
#define IM_V 147456u
#define SM_WB 155648u
#define SM_CS SM_WB
#define SM_KVT 158720u
#define SM_B2 163072u
#define SM_DUMP 163392u
#define SM_SYNC 163584u
#define SY_KV (SM_SYNC)
#define SY_FREE (SM_SYNC + 8u)
#define SY_TF (SM_SYNC + 12u)
#define SY_W (SM_SYNC + 16u)
#define SY_IM (SM_SYNC + 32u)
#define SY_G (SM_SYNC + 40u)
#define SY_U (SM_SYNC + 48u)
#define SY_I (SM_SYNC + 56u)
#define SY_A (SM_SYNC + 64u)
#define SY_H (SM_SYNC + 68u)
#define SY_B (SM_SYNC + 72u)
#define SY_X (SM_SYNC + 80u)
#define SY_Y (SM_SYNC + 88u)
#define LDS_BYTES 163840u
#define SPIN_CAP 60000
constexpr int MWIN = 12;
constexpr int GD = 4, MD = 2;
constexpr int NE = 2;

constexpr int p1_ex(int k) { return (k == -1 ? 2 : 0) + (k == 4) + 2 * (k == 3); }
constexpr int p1_cnt(int cp) { return (cp == 6 ? p1_ex(4) - 1 : p1_ex(cp - 2)) + 4 + p1_ex(cp - 1); }
template <int N> struct IC { static constexpr int v = N; };
template <int I, int N, class F> DEV void sfor(F&& f) { if constexpr (I < N) { f(IC<I>{}); sfor<I + 1, N>(f); } }
struct Params {
  const char* x; const char* wqkv; const char* w1; const char* wphi; const char* b1;
  const char* wo; const char* w2; const char* b2; char* out; char* ws;
};

DEV v4u mk_srd(const void* p, u32 bytes) {
  const u64 a = (u64)p;
  return (v4u){(u32)a, (u32)(a >> 32) & 0xffffu, bytes, 0x00020000u};
}
#define DMA_TXT(v, r, so, o) "buffer_load_dwordx4 " v ", " r ", " so " offen offset:" o " lds"
template <int OFF> DEV void dma(v4u srd, u32 voff, u32 soff) {
  asm volatile(DMA_TXT("%0", "%1", "%2", "%3") :: "v"(voff), "s"(srd), "s"(soff), "n"(OFF) : "memory");
}
template <u32 LDS> DEV void dma_nx(v4u srd, u32 voff, u32& kx, u32& kw, u32 whalf, u32 base) {
  asm volatile("s_add_u32 m0, %2, %3\n\ts_add_u32 %0, %0, 0x80\n\ts_and_b32 %0, %0, 0x3ff\n\t" DMA_TXT("%5", "%6", "%0", "0") "\n\ts_mul_i32 %1, %0, %4"
               : "+s"(kx), "=s"(kw) : "s"(base), "n"(LDS), "s"(whalf), "v"(voff), "s"(srd) : "scc", "memory");
}
template <u32 LDS> DEV void dma_nw(v4u srd, u32 voff, u32& kx, u32& kw, u32 whalf, u32 base) {
  asm volatile("s_add_u32 m0, %2, %3\n\ts_add_u32 %0, %0, 0x80\n\ts_and_b32 %0, %0, 0x3ff\n\ts_mul_i32 %1, %0, %4\n\t" DMA_TXT("%5", "%6", "%1", "0")
               : "+s"(kx), "=&s"(kw) : "s"(base), "n"(LDS), "s"(whalf), "v"(voff), "s"(srd) : "scc", "memory");
}
template <int E> DEV void dma_grp(v4u srd, u32 voff, u32& gs, u32 lds, u32 mem) {
  if constexpr (E == 0) asm volatile("s_mov_b32 m0, %1\n\ts_mov_b32 %0, %2\n\t" DMA_TXT("%3", "%4", "%0", "0") : "=&s"(gs) : "s"(lds), "s"(mem), "v"(voff), "s"(srd) : "memory");
  else asm volatile("s_add_u32 %0, %0, 0x1c00\n\t" DMA_TXT("%1", "%2", "%0", "%3") : "+s"(gs) : "v"(voff), "s"(srd), "n"(E * 1024) : "scc", "memory");
}
template <int OFF> DEV void ld16o(v4u& d, v4u srd, u32 voff, u32 soff) {
  asm volatile("buffer_load_dwordx4 %0, %1, %2, %3 offen offset:%4 sc1" : "=v"(d) : "v"(voff), "s"(srd), "s"(soff), "n"(OFF) : "memory");
}
DEV void ld4(u32& d, v4u srd, u32 voff, u32 soff) { asm volatile("buffer_load_dword %0, %1, %2, %3 offen" : "+v"(d) : "v"(voff), "s"(srd), "s"(soff) : "memory"); }
DEV void ld8_sc1(v2u& d, v4u srd, u32 voff) { asm volatile("buffer_load_dwordx2 %0, %1, %2, 0 offen sc1" : "=v"(d) : "v"(voff), "s"(srd) : "memory"); }
DEV void hold(v4u a, v4u c) { asm volatile("s_nop 1" :: "v"(a), "v"(c) : "memory"); }
template <class T> DEV void pin(T& v) { asm volatile("" : "+v"(v)); }
template <class T> DEV void pin4(T& a, T& b, T& c, T& d) { asm volatile("" : "+v"(a), "+v"(b), "+v"(c), "+v"(d)); }
DEV void dma16m(v4u srd, u32 voff, u32 dst, u32 idle) {
  asm volatile("s_mov_b32 m0, %0\n\ts_lshr_b64 exec, -1, %3\n\tbuffer_load_dwordx4 %1, %2, 0 offen lds\n\ts_mov_b64 exec, -1" :: "s"(dst), "v"(voff), "s"(srd), "s"(idle) : "scc", "memory");
}
#define FENCE() __builtin_amdgcn_sched_barrier(0)
template <int N> DEV void bar_all() { FENCE(); asm volatile("s_waitcnt vmcnt(%0) lgkmcnt(0)\n\ts_barrier" :: "n"(N) : "memory"); FENCE(); }
template <int N> DEV void wait_vm() { FENCE(); asm volatile("s_waitcnt vmcnt(%0)" :: "n"(N) : "memory"); FENCE(); }
#define PRIO(n) __builtin_amdgcn_s_setprio(n)
#define RARE(c) __builtin_expect(__builtin_amdgcn_ballot_w64(c) != 0ull, 0)
#define OPQ(x) pin(x)

DEV v4u lds16(u32 a) { return *(LAS v4u*)a; }
DEV v2u ldstr(u32 a) { return __builtin_bit_cast(v2u, __builtin_amdgcn_ds_read_tr16_b64_v4i16((LAS s4*)a)); }
DEV v4u ldsfrag(u32 a, u32 d) { const v2u p = ldstr(a), q = ldstr(a + d); return (v4u){p.x, p.y, q.x, q.y}; }
DEV f16v mfma32(v4u a, v4u b, f16v c) { f16v d = __builtin_amdgcn_mfma_f32_32x32x16_bf16(__builtin_bit_cast(bfx8, a), __builtin_bit_cast(bfx8, b), c, 0, 0, 0); OPQ(d); return d; }
DEV f4 mfma16(v4u a, v4u b, f4 c) { f4 d = __builtin_amdgcn_mfma_f32_16x16x32_bf16(__builtin_bit_cast(bfx8, a), __builtin_bit_cast(bfx8, b), c, 0, 0, 0); OPQ(d); return d; }
DEV u32 pk(float a, float b) { return __builtin_bit_cast(u32, __builtin_convertvector((f2){a, b}, bfx2)); }
DEV float blo(u32 v) { return __uint_as_float(v << 16); }
DEV float bhi(u32 v) { return __uint_as_float(v & 0xffff0000u); }
DEV v4u pk8(const f16v& a, int o) { return (v4u){pk(a[o], a[o + 1]), pk(a[o + 2], a[o + 3]), pk(a[o + 4], a[o + 5]), pk(a[o + 6], a[o + 7])}; }
extern "C" __device__ float mul1(float, float) __asm("llvm.amdgcn.fmul.legacy");

DEV u32 opq(u32 v) { OPQ(v); return v; }
DEV u32 split2(float v) { return pk(v, v - bhi(pk(v, v))); }
DEV v4u fold_w(f4 c, u32 bb) {
  const float cs = ((c[0] + c[1]) + c[2]) + c[3];
  const u32 h = pk(cs, cs);
  const float l = cs - bhi(h);
  return (v4u){h, pk(l, l), bb, 0u};
}
DEV float half_sum(float v) {
  v += __uint_as_float(__builtin_amdgcn_update_dpp(0u, __float_as_uint(v), 0x121, 0xf, 0xf, false));
  v += __uint_as_float(__builtin_amdgcn_update_dpp(0u, __float_as_uint(v), 0x122, 0xf, 0xf, false));
  v += __uint_as_float(__builtin_amdgcn_update_dpp(0u, __float_as_uint(v), 0x124, 0xf, 0xf, false));
  v += __uint_as_float(__builtin_amdgcn_update_dpp(0u, __float_as_uint(v), 0x128, 0xf, 0xf, false));
  const v2u s2 = __builtin_amdgcn_permlane16_swap(__float_as_uint(v), __float_as_uint(v), false, false);
  return __uint_as_float(s2.x) + __uint_as_float(s2.y);
}
DEV float max16(const f16v& p) {
  float mx = fmaxf(fmaxf(p[0], p[1]), p[2]);
#pragma unroll
  for (int i = 3; i < 15; i += 2) mx = fmaxf(fmaxf(mx, p[i]), p[i + 1]);
  return fmaxf(mx, p[15]);
}
template <bool PK, class H> DEV void softmax16(const f16v& p, float mx, float c, v4u& o0, v4u& o1, H&& hook) {
  const float nm = -mx * c;
  float e[16], s0 = 0.f;
  f2 s2 = {0.f, 0.f};
  sfor<0, 4>([&](auto q) {
    hook(q);
#pragma unroll
    for (int i = 4 * q.v; i < 4 * q.v + 4; i += 2) {
      if constexpr (PK) {
        const f2 a = __builtin_elementwise_fma((f2){p[i], p[i + 1]}, (f2){c, c}, (f2){nm, nm});
        e[i] = __builtin_amdgcn_exp2f(a.x); e[i + 1] = __builtin_amdgcn_exp2f(a.y);
        s2 = i ? s2 + (f2){e[i], e[i + 1]} : (f2){e[i], e[i + 1]};
      } else {
        e[i] = __builtin_amdgcn_exp2f(__builtin_fmaf(p[i], c, nm));
        e[i + 1] = __builtin_amdgcn_exp2f(__builtin_fmaf(p[i + 1], c, nm));
        s0 = i ? (s0 + e[i]) + e[i + 1] : e[i] + e[i + 1];
      }
    }
    if constexpr (PK) OPQ(s2); else OPQ(s0);
  });
  float rs = __builtin_amdgcn_rcpf(PK ? s2.x + s2.y : s0);
  OPQ(rs);
  sfor<0, 4>([&](auto q) {
    hook(IC<4 + q.v>{});
    u32 w2[2];
#pragma unroll
    for (int j = 0; j < 2; ++j) {
      const int i = 4 * q.v + 2 * j;
      if constexpr (PK) { const f2 a = (f2){e[i], e[i + 1]} * (f2){rs, rs}; w2[j] = pk(a.x, a.y); }
      else w2[j] = pk(mul1(e[i], rs), mul1(e[i + 1], rs));
    }
    pin(w2[0]); pin(w2[1]);
    v4u& o = q.v < 2 ? o0 : o1;
    o[(q.v & 1) * 2] = w2[0]; o[(q.v & 1) * 2 + 1] = w2[1];
  });
}
template <class H> DEV void gelu16(const f16v& a, float c0, float c1, float sig, v4u& o0, v4u& o1, H&& hook) {
  sfor<0, 4>([&](auto q) {
    hook(IC<2 * q.v>{});
    float e[4], g[4];
#pragma unroll
    for (int j = 0; j < 4; j += 2) {
      const f2 v = {a[4 * q.v + j], a[4 * q.v + j + 1]};
      e[j] = __builtin_amdgcn_exp2f(v.x * __builtin_fmaf(v.x * v.x, c1, c0));
      e[j + 1] = __builtin_amdgcn_exp2f(v.y * __builtin_fmaf(v.y * v.y, c1, c0));
    }
    pin4(e[0], e[1], e[2], e[3]);
    hook(IC<2 * q.v + 1>{});
#pragma unroll
    for (int j = 0; j < 4; j += 2) {
      const f2 v = {a[4 * q.v + j], a[4 * q.v + j + 1]};
      g[j] = mul1(v.x, __builtin_amdgcn_rcpf(__builtin_fmaf(e[j], sig, sig)));
      g[j + 1] = mul1(v.y, __builtin_amdgcn_rcpf(__builtin_fmaf(e[j + 1], sig, sig)));
    }
    v4u& o = q.v < 2 ? o0 : o1;
    u32 pa = pk(g[0], g[1]), pb = pk(g[2], g[3]);
    pin(pa); pin(pb);
    o[(q.v & 1) * 2] = pa; o[(q.v & 1) * 2 + 1] = pb;
  });
}
DEV void wait_flags(__amdgpu_buffer_rsrc_t rs, u32 voff, u32 tag, u32 tag2) {
#pragma unroll 1
  for (int sp = 0; sp < SPIN_CAP; ++sp) {
    const v2u f = __builtin_amdgcn_raw_buffer_load_b64(rs, voff, 0, 16);
    if (__all(f.x == tag && f.y == tag2)) break;
    asm volatile("s_sleep 2" ::: "memory");
  }
}
DEV void end_wave() { asm volatile("s_endpgm" ::: "memory"); }
DEV void spin_flags(v2u& f, v4u srd, u32 voff, u64 tg) {
  u32 n;
  asm volatile("v_cmp_ne_u64 vcc, %4, %0\n\ts_mov_b32 %1, %5\n\ts_cbranch_vccz 2f\n"
               "1:\n\ts_mov_b64 exec, vcc\n\ts_sleep %6\n\tbuffer_load_dwordx2 %0, %2, %3, 0 offen sc1\n\ts_sub_u32 %1, %1, 1\n\ts_waitcnt vmcnt(0)\n\tv_cmp_ne_u64 vcc, %4, %0\n\t"
               "s_cbranch_scc1 3f\n\ts_cbranch_vccnz 1b\n3:\n\ts_mov_b64 exec, -1\n2:"
               : "+v"(f), "=&s"(n) : "v"(voff), "s"(srd), "s"(tg), "n"(SPIN_CAP), "n"(4) : "vcc", "scc", "memory");
}
template <int OFF> DEV void spin_written(v4u srd, u32 voff, u32 soff) {
  u32 n, f;
  asm volatile("s_mov_b32 %1, %6\n"
               "1:\n\tbuffer_load_dword %0, %2, %3, %4 offen offset:%5 sc1\n\ts_sub_u32 %1, %1, 1\n\ts_waitcnt vmcnt(0)\n\tv_cmp_eq_u32 vcc, -1, %0\n\t"
               "s_cbranch_scc1 2f\n\ts_cbranch_vccnz 1b\n2:"
               : "=&v"(f), "=&s"(n) : "v"(voff), "s"(srd), "s"(soff), "n"(OFF), "n"(SPIN_CAP) : "vcc", "scc", "memory");
}
DEV void spin_lds(u32 a) {
  u32 n, f;
  asm volatile("s_mov_b32 %1, 400000\n"
               "1:\n\tds_read_b32 %0, %2\n\ts_sub_u32 %1, %1, 1\n\ts_waitcnt lgkmcnt(0)\n\tv_cmp_ne_u32 vcc, 0x01010101, %0\n\t"
               "s_cbranch_scc1 2f\n\ts_cbranch_vccnz 1b\n2:"
               : "=&v"(f), "=&s"(n) : "v"(a) : "vcc", "scc", "memory");
}
template <int OFF> DEV void spin_get(u32 fa, u32 a, f4* v) {
  u32 n, f;
  asm volatile("s_mov_b32 %1, 400000\n"
               "1:\n\tds_read_u8 %0, %6\n\tds_read_b128 %2, %7 offset:%8\n\tds_read_b128 %3, %7 offset:%9\n\tds_read_b128 %4, %7 offset:%10\n\t"
               "ds_read_b128 %5, %7 offset:%11\n\ts_sub_u32 %1, %1, 1\n\ts_waitcnt lgkmcnt(4)\n\tv_cmp_ne_u32 vcc, 1, %0\n\t"
               "s_cbranch_scc1 2f\n\ts_cbranch_vccnz 1b\n2:\n\ts_waitcnt lgkmcnt(0)"
               : "=&v"(f), "=&s"(n), "=&v"(v[0]), "=&v"(v[1]), "=&v"(v[2]), "=&v"(v[3])
               : "v"(fa), "v"(a), "n"(OFF), "n"(OFF + 1024), "n"(OFF + 2048), "n"(OFF + 3072) : "vcc", "scc", "memory");
}
template <int OFF> DEV void spin_get2(u32 fa, u32 fb, u32 a, u32 b, f4* v, f4* w) {
  u32 n, f, g;
  asm volatile("s_mov_b32 %2, 400000\n"
               "1:\n\tds_read_u8 %0, %11\n\tds_read_u8 %1, %12\n\tds_read_b128 %3, %13 offset:%15\n\tds_read_b128 %4, %13 offset:%16\n\tds_read_b128 %5, %13 offset:%17\n\t"
               "ds_read_b128 %6, %13 offset:%18\n\tds_read_b128 %7, %14\n\tds_read_b128 %8, %14 offset:1024\n\tds_read_b128 %9, %14 offset:2048\n\tds_read_b128 %10, %14 offset:3072\n\t"
               "s_sub_u32 %2, %2, 1\n\ts_waitcnt lgkmcnt(8)\n\tv_and_b32 %0, %0, %1\n\tv_cmp_ne_u32 vcc, 1, %0\n\ts_waitcnt lgkmcnt(0)\n\t"
               "s_cbranch_scc1 2f\n\ts_cbranch_vccnz 1b\n2:"
               : "=&v"(f), "=&v"(g), "=&s"(n), "=&v"(v[0]), "=&v"(v[1]), "=&v"(v[2]), "=&v"(v[3]), "=&v"(w[0]), "=&v"(w[1]), "=&v"(w[2]), "=&v"(w[3])
               : "v"(fa), "v"(fb), "v"(a), "v"(b), "n"(OFF), "n"(OFF + 1024), "n"(OFF + 2048), "n"(OFF + 3072) : "vcc", "scc", "memory");
}
DEV void lds_arrive(u32 a) { asm volatile("s_waitcnt lgkmcnt(0)" ::: "memory"); *(volatile LAS unsigned char*)a = 1; }
DEV u32 lds_peek4(u32 a) { return *(volatile LAS u32*)a; }
DEV u32 lds_peek8(u32 a) { const v2u f = *(volatile LAS v2u*)a; return f.x & f.y; }
template <int N> DEV void lds_wait(u32 a, u32 f) {
  if (RARE(f != 0x01010101u)) {
#pragma unroll 1
    for (int sp = 0; sp < 400000; ++sp) {
      f = N == 4 ? lds_peek4(a) : lds_peek8(a);
      if (!__builtin_amdgcn_ballot_w64(f != 0x01010101u)) break;
      asm volatile("s_sleep 1" ::: "memory");
    }
  }
  asm volatile("" ::: "memory");
}

__global__ void __launch_bounds__(512, 2) hh_fused(Params p) {
  const u32 tid = threadIdx.x, lane = tid & 63u;
  const u32 w = __builtin_amdgcn_readfirstlane(tid >> 6);
  u32 b = blockIdx.x;
  u64 px0, pw0, pws0, psm0, pwo0, pw20, pout0, px, pw, pws;
  asm volatile("s_load_dwordx2 %0, %8, 0x0\n\ts_load_dwordx2 %1, %8, %9\n\ts_load_dwordx2 %2, %8, 0x48\n\ts_load_dwordx2 %3, %8, %10\n\t"
               "s_load_dwordx2 %4, %8, 0x28\n\ts_load_dwordx2 %5, %8, 0x30\n\ts_load_dwordx2 %6, %8, 0x40"
               : "=&s"(px0), "=&s"(pw0), "=&s"(pws0), "=&s"(psm0), "=&s"(pwo0), "=&s"(pw20), "=&s"(pout0), "+s"(b)
               : "s"(__builtin_amdgcn_kernarg_segment_ptr()), "s"(8u + ((b >> 3) & 8u)), "s"(w == 7u ? 0x38u : 0x18u + ((b >> 3) & 8u)));
  u32 pd0;
  {
    u64 pc; u32 len;
    asm volatile("s_getpc_b64 %0\n.Lhh_pc:\n\ts_mov_b32 %1, .Lhh_end-.Lhh_pc\n\t.subsection 1\n.Lhh_end:\n\t.subsection 0" : "=s"(pc), "=s"(len));
    const u32 vo = (lane >> 1) * 65536u + (lane & 1u) * 128u;
    asm volatile("buffer_load_dword %0, %1, %2, %3 offen" : "=v"(pd0) : "v"(vo), "s"(mk_srd((const void*)pc, len)), "s"(((b & ~7u) + w) * 256u) : "memory");
    asm volatile("" : "+s"(b));
  }
  const u32 xcd = b & 7u, rb = (b >> 3) & 31u, xg = xcd >> 1, gl = rb >> 3;
  const u32 isM = gl & 1u;
  u32 idx = xg * 2u + (gl >> 1);
  u32 t = (xcd & 1u) * 8u + (rb & 7u);
  const u32 koff = (2u * (rb + gl) + xg + xcd) & 7u;
  const u32 hh = lane >> 5, l31 = lane & 31u, q4 = lane & 3u, rr = (lane >> 2) & 3u, g1 = (lane >> 4) & 1u;
  const u32 whalf = isM ? 1376u : 1536u, wstr = 2u * whalf;
  const u32 row7 = (lane >> 2) & 7u;
  u32 vx0 = (16u * w + row7) * 1024u + hh * 64u + 16u * (q4 ^ (row7 >> 2));
  u32 vx1 = ((vx0 + 8192u) ^ 32u) - 1024u;
  const u32 colb = isM ? idx * 256u : 1024u + idx * 128u;
  u32 vw0 = (8u * w + rr) * wstr + colb + g1 * 64u + hh * (isM ? 128u : 1024u) + 16u * q4 + 2048u;
  u32 vw1 = vw0 + 4u * wstr - 1024u;
  const u32 w4k = w * 4096u;
  u32 kx = koff * 128u, kw = kx * whalf, xs = t * 131072u, wn = 512u * wstr + 4096u;
  u32 srow = (idx * 2u + isM) * 8u + 2u * w;
  u32 vrow = hh * 1024u + l31 * 16u, ep0, ep;
  asm volatile("" : "+v"(vx0), "+v"(vx1), "+v"(vw0), "+v"(vw1), "+v"(vrow));
  asm volatile("s_or_b32 m0, %7, 0x10000\n\ts_waitcnt lgkmcnt(0)\n\ts_load_dword %3, %6, 0x0\n\ts_mov_b64 %0, %4\n\ts_mov_b64 %1, %5\n\ts_mov_b64 %2, %6"
               : "=&s"(px), "=&s"(pw), "=&s"(pws), "=&s"(ep0) : "s"(px0), "s"(pw0), "s"(pws0), "s"(w4k), "s"(xs), "s"(wn), "s"(kx), "s"(kw), "s"(srow * 1024u) : "scc");
  const v4u srdX = mk_srd((const char*)px + xs, 131072u);
  v4u xra, xrb;
  asm volatile("s_cmp_gt_u32 %5, 3\n\ts_cbranch_scc1 1f\n\tbuffer_load_dwordx4 %0, %2, %3, %4 offen\n\tbuffer_load_dwordx4 %1, %2, %3, %4 offen offset:512\n1:"
               : "=&v"(xra), "=&v"(xrb) : "v"(vrow), "s"(srdX), "s"(srow * 1024u), "s"(w) : "scc", "memory");
  const v4u srdW = mk_srd((const char*)pw - 4096, wn);
  u32 vu;
  auto dq = [&](auto qc) {
    constexpr int P = qc.v >> 2, e = qc.v & 3;
    if constexpr (P < 8) {
      if constexpr (e == 0) {
        if constexpr (P > 0) dma_nx<SLOT((P + 2) & 3)>(srdX, vx0, kx, kw, whalf, w4k);
        else dma<0>(srdX, vx0, kx);
      } else if constexpr (e == 1) dma<2048>(srdW, vw0, kw);
      else if constexpr (e == 2) dma<1024>(srdX, vx1, kx);
      else dma<3072>(srdW, vw1, kw);
    } else {
      dma_nw<SLOT((P + 2) & 3) + e * 1024>(srdW, vu, kx, kw, whalf, w4k);
    }
  };
  sfor<0, 4>(dq);
  u64 psm, pwo, pw2, pout;
  u32 wv = w;
  asm volatile("s_barrier\n\ts_mov_b64 %0, %7\n\ts_mov_b64 %1, %8\n\ts_mov_b64 %2, %9\n\ts_mov_b64 %3, %10"
               : "=&s"(psm), "=&s"(pwo), "=&s"(pw2), "=&s"(pout), "+s"(idx), "+s"(t), "+s"(wv) : "s"(psm0), "s"(pwo0), "s"(pw20), "s"(pout0) : "memory");
  const u32 r = wv & 3u, hf = wv >> 2;
  const u32 six = idx < 3u;
  const u32 seg2 = six ? 32u + 2u * idx : 35u + idx;
  const u32 tb = (xcd & 1u) * 16u + xg * 4u + (rb & 3u), nb = rb >> 2;
  const __amdgpu_buffer_rsrc_t rsW = __builtin_amdgcn_make_buffer_rsrc((void*)pws, 0, WS_END, 0x00020000);
  const v4u srdS = mk_srd((const void*)pws, WS_END);
  v4u srdW2 = mk_srd((const void*)pw2, 1376u * 1024u), srdWo = mk_srd((const void*)pwo, 512u * 1024u);
  asm volatile("" : "+s"(srdW2), "+s"(srdWo));
  const u32 l16 = opq(lane * 16u);
  if (hf) {
    const u32 lo = w == 7u ? nb * 128u : isM ? idx * 256u : idx * 2048u + (~w & 1u) * 1024u;
    const u32 hi = isM ? (seg2 + six) * 64u - 320u : lo;
    dma16m(mk_srd((const void*)psm, w == 7u ? 1024u : isM ? 2752u : 16384u), l16 + (lane < 16u ? lo : hi),
           w == 7u ? SM_B2 : SM_WB + (~w & 1u) * 1024u, w - 5u < 2u ? 0u : 56u);
  }
  const u32 rot = r ^ (2u * hf);
  const u32 xa0 = (2u * r + g1) * 4096u + ((l31 >> 3) & 1u) * 1024u + (l31 & 7u) * 64u + ((16u * (hh ^ ((l31 >> 2) & 3u))) ^ ((rot & 1u) * 32u)) + ((rot >> 1) * 512u + SLOT(2));
  const u32 wl = rr * 64u + g1 * 32u + q4 * 8u;
  const u32 wa0 = (rot * 8192u + hf * 256u + SLOT(2)) + hh * 4096u + wl;
  u32 xa[4], wa[4];
#pragma unroll
  for (u32 s = 0; s < 4; ++s) { xa[s] = xa0 ^ ((s >> 1) * 512u + (s & 1u) * 32u); wa[s] = wa0 ^ (s * 8192u); }
  const u32 one2 = 0x3f803f80u;
  const u32 selv = ((lane & 15u) == (g1 ? 4u : 0u)) ? one2 : 0u;
  v4u sel = {selv, selv, selv, selv};
  asm volatile("" : "+v"(xa[0]), "+v"(xa[1]), "+v"(xa[2]), "+v"(xa[3]), "+v"(wa[0]), "+v"(wa[1]), "+v"(wa[2]), "+v"(wa[3]), "+v"(sel));
  vu = opq((8u * w + 4u * hh + rr) * wstr + g1 * (isM && !six ? 0u : 64u) + 16u * q4 + ((isM ? seg2 * 64u : idx * 128u) + 4096u));
  u32 vp = opq((4u * hh + rr) * 1024u + nb * 128u + g1 * 64u + 16u * q4);
  const bool low = lane < 32u;
  u32 csr = opq(l31 * 48u + (SM_CS + hf * 1536u)), csw = opq(low ? csr + r * 4u : SM_DUMP);
  u32 sma = opq(SM_WB + opq(hf * 64u + g1 * 32u + q4 * 8u + (rr >> 1) * 128u) * isM + opq(hf * 1024u + (4u * hh + rr) * 32u + (2u * g1 + (q4 >> 1)) * 8u) * (1u - isM));
  u32 sgn = opq(((lane >> 2) & 1u) * (isM ? 0u : 0x80008000u));
  const u32 mrow = wv & 1u, kh = (wv >> 1) & 1u;
  u32 pa0 = opq(hh * 512u + rr * 64u + g1 * 32u + (q4 & 1u) * 16u + (q4 >> 1) * 8u + ((hf ? 0u : 27u * 4096u) + kh * (hf ? 16640u : 10496u)));
  const u32 tok32 = 4u * t + r;
  const u32 un = (r >> 1) * 4u + 2u * hf + (r & 1u);
  const u32 pi = (idx * 8u + un) * 16u + t;
  const u32 vc1 = opq(l16 + (lane < 32u ? WS_KVP + pi * 512u : lane < 34u ? WS_ZP + pi * 32u - 512u : 0x80000000u));
  u32 ad[8];
  {
    const u32 sw = (l31 >> 2) & 1u, tk32 = (32u * r + l31) * 32u;
    const u32 la = (16u * hh + 4u * g1 + rr) * 32u + ((q4 * 8u) ^ (g1 * 16u));
    ad[0] = opq(lane * 8u);
    ad[1] = opq(((lane & 15u) << 24) + (lane >> 4) * 8u);
    ad[2] = opq(IM_PHI + hh * 4096u + tk32 + sw * 16u);
    ad[3] = opq(ad[2] ^ 16u);
    ad[4] = opq(IM_V + tk32 + (hh ^ sw) * 16u);
    ad[5] = opq(la + (IM_PHI + ((r & 1u) * 2u + (r >> 1)) * 4096u));
    ad[6] = opq(la + (IM_V + (r & 1u) * 4096u));
    ad[7] = opq((lane & 15u) * 8u + (WS_FC + 1024u + idx * 128u));
  }
  const u32 gw = b * 8u + w;
  v4u srdP = gw < 1376u ? srdW2 : srdWo;
  const u32 vpw = opq((lane & 7u) * 128u + ((lane >> 3) << 24));
  u32 spw = (gw < 1376u ? gw : gw - 1376u) * 1024u;
  const u32 vpu = opq((lane & 7u) * wstr + ((lane >> 3) & 1u) * (isM && !six ? 60u : 124u) + ((lane >> 4) << 24));
  u32 spu = ((rb & 7u) * 8u + w) * 8u * wstr + (isM ? seg2 * 64u : idx * 128u) + 4096u;
  asm volatile("" : "+s"(srdP), "+s"(spw), "+s"(spu));
  FENCE();
  u32 rmu, rsg;
  asm volatile("" : "=v"(rmu), "=v"(rsg));
  if (!hf) {
    asm volatile("s_waitcnt vmcnt(4)" : "+v"(xra), "+v"(xrb) :: "memory");
    f2 xv[8];
#pragma unroll
    for (int i = 0; i < 4; ++i) { xv[i] = (f2){blo(xra[i]), bhi(xra[i])}; xv[4 + i] = (f2){blo(xrb[i]), bhi(xrb[i])}; }
    const f2 s2 = ((xv[0] + xv[1]) + (xv[2] + xv[3])) + ((xv[4] + xv[5]) + (xv[6] + xv[7]));
    const float nmu = half_sum(s2.x + s2.y) * (-1.f / 512.f);
    f2 q2 = {0.f, 0.f};
#pragma unroll
    for (int i = 0; i < 8; ++i) { const f2 d = xv[i] + (f2){nmu, nmu}; q2 = __builtin_elementwise_fma(d, d, q2); }
    const float var = half_sum(q2.x + q2.y) * (1.f / 512.f);
    rmu = split2(nmu);
    rsg = __float_as_uint(__builtin_amdgcn_sqrtf(l31 ? __builtin_fmaf(1e-5f, var + 1e-5f, var) : var + 1e-5f));
  }
  asm volatile("s_waitcnt lgkmcnt(0)\n\ts_mov_b32 %0, %3" : "=s"(ep), "+v"(rmu), "+v"(rsg) : "s"(ep0) : "memory");
  const u32 tag = ep * 1664525u + 1013904223u, tag2 = (ep ^ 0x9e3779b9u) * 2654435761u + 12345u;
  sfor<4, 8>(dq);
  {
    v4u rc = {rmu, tag, rsg, tag2}, ff = {~0u, ~0u, ~0u, ~0u};
    const u32 vr = opq((l31 & 1u) * 32768u + hh * 16u + ((l31 >> 1) << 24));
    asm volatile("" : "+v"(rc), "+v"(ff));
    __builtin_amdgcn_raw_buffer_store_b128(rc, rsW, vr, (WS_ST + (t * 128u + srow) * 16u) | (hf << 31), 16);
    __builtin_amdgcn_raw_buffer_store_b128(ff, rsW, vc1, isM << 30, 16);
    hold(rc, ff);
  }
  if (tid < 32u) *(LAS u32*)(SM_SYNC + tid * 4u) = 0u;

  v4u xr[32];
  v2u wf[16];
  f16v acc0, acc1;
  f4 cs0 = {0.f, 0.f, 0.f, 0.f}, cs1 = {0.f, 0.f, 0.f, 0.f};
#pragma unroll
  for (int i = 0; i < 16; ++i) { acc0[i] = 0.f; acc1[i] = 0.f; }
  const u32 vst = opq(l16 & 511u), sst = WS_ST + isM * 32768u + (t * 128u + r * 32u) * 16u;
  v4u aphi[2], rec, fx;
  float rinv, sig;
  auto frag = [&](int s, int u) { const v2u a = wf[s * 4 + u * 2], c = wf[s * 4 + u * 2 + 1]; return (v4u){a.x, a.y, c.x, c.y}; };
  sfor<0, 8>([&](auto cpc) {
    constexpr int cp = cpc.v;
    constexpr u32 sl = SLOT(cp & 1);
    auto rx = [&](int s) { xr[cp * 4 + s] = lds16(xa[s] + sl); };
    auto rw = [&](int s, int u, int h) { wf[s * 4 + u * 2 + h] = ldstr(wa[s] + sl + 2048u + u * 512u + h * 1024u); };
    bar_all<p1_cnt(cp)>();
    auto flip = [&](int s) {
      if constexpr (cp & 1) { wa[s] = opq(wa[s] ^ 65536u); if constexpr (cp < 7) xa[s] = opq(xa[s] ^ 65536u); }
    };
    FENCE();
    if constexpr (cp > 0) acc0 = mfma32(frag(2, 0), xr[cp * 4 - 2], acc0);
    FENCE();
    rx(0); rw(0, 0, 0); rw(0, 0, 1);
    FENCE();
    if constexpr (cp > 0) acc1 = mfma32(frag(2, 1), xr[cp * 4 - 2], acc1);
    FENCE();
    rw(0, 1, 0); rw(0, 1, 1); rx(1);
    if constexpr (cp > 0) { FENCE(); dq(IC<cp * 4 + 8>{}); }
    FENCE();
    if constexpr (cp > 0) acc0 = mfma32(frag(3, 0), xr[cp * 4 - 1], acc0);
    FENCE();
    rw(1, 0, 0); rw(1, 0, 1); rw(1, 1, 0);
    flip(0);
    FENCE();
    if constexpr (cp > 0) acc1 = mfma32(frag(3, 1), xr[cp * 4 - 1], acc1);
    FENCE();
    rw(1, 1, 1); rw(2, 0, 0); rw(2, 0, 1); rx(2);
    if constexpr (cp > 0) { FENCE(); dq(IC<cp * 4 + 9>{}); }
    FENCE();
    if constexpr (cp == 7) { cs0 = mfma16(sel, frag(0, 0), cs0); cs1 = mfma16(sel, frag(0, 1), cs1); }
    acc0 = mfma32(frag(0, 0), xr[cp * 4], acc0);
    FENCE();
    rw(2, 1, 0); rw(2, 1, 1); rx(3);
    flip(1);
    if constexpr (cp == 2) { aphi[0] ^= (v4u){sgn, sgn, sgn, sgn}; OPQ(aphi[0]); }
    if constexpr (cp == 6) {
      OPQ(rec);
      if (RARE(((rec[1] ^ tag) | (rec[3] ^ tag2)) != 0u)) {
#pragma unroll 1
        for (int sp = 0; sp < SPIN_CAP; ++sp) {
          rec = __builtin_amdgcn_raw_buffer_load_b128(rsW, vst, sst, 16);
          if (__all(rec[1] == tag && rec[3] == tag2)) break;
          asm volatile("s_sleep 2" ::: "memory");
        }
      }
    }
    FENCE();
    acc1 = mfma32(frag(0, 1), xr[cp * 4], acc1);
    FENCE();
    rw(3, 0, 0); rw(3, 0, 1); rw(3, 1, 0); rw(3, 1, 1);
    if constexpr (cp > 0) { FENCE(); dq(IC<cp * 4 + 10>{}); }
    FENCE();
    if constexpr (cp < 7) { cs0 = mfma16(sel, frag(0, 0), cs0); cs1 = mfma16(sel, frag(0, 1), cs1); }
    FENCE();
    flip(2);
    if constexpr (cp == 2) { aphi[1] ^= (v4u){sgn, sgn, sgn, sgn}; OPQ(aphi[1]); }
    if constexpr (cp == 6) { sig = __uint_as_float(rec[2]); rinv = __builtin_amdgcn_rcpf(sig); OPQ(rinv); }
    if constexpr (cp == 7) { *(LAS float*)(csw) = cs0[0]; *(LAS float*)(csw + 16u) = cs1[0]; }
    FENCE();
    acc0 = mfma32(frag(1, 0), xr[cp * 4 + 1], acc0);
    FENCE();
    flip(3);
    if constexpr (cp == 6) {
      const u32 m2 = low ? rec[0] : 0u, s2 = low ? split2(sig) : 0u;
      fx = (v4u){m2, m2, s2, 0u};
      OPQ(fx);
    }
    if constexpr (cp > 0) { FENCE(); dq(IC<cp * 4 + 11>{}); }
    if constexpr (cp == 4) ld16o<0>(rec, srdS, vst, sst);
    FENCE();
    acc1 = mfma32(frag(1, 1), xr[cp * 4 + 1], acc1);
    if constexpr (cp == 0) { FENCE(); sfor<8, 12>(dq); }
    if constexpr (cp == 1) { FENCE(); aphi[0] = ldsfrag(sma, 256u); aphi[1] = ldsfrag(sma + 512u, 256u); }
    if constexpr (cp == 3) { FENCE(); ld4(pd0, srdW, vpu, spu); FENCE(); ld4(pd0, srdP, vpw, spw); }
    FENCE();
  });
  u32 gs;
  auto wdma = [&](auto ec, v4u srd, u32 lds, u32 mem) {
    FENCE();
    dma_grp<ec.v>(srd, vp, gs, lds, mem);
    FENCE();
  };
  u32 tidv = tid, l16v = l16;
  OPQ(tidv);
  auto tail = [&](auto ismc, auto hfc) {
  constexpr bool ISM = decltype(ismc)::v; constexpr u32 hf = decltype(hfc)::v;
  const u32 tid = tidv, lane = tid & 63u;
  const u32 hh = lane >> 5, l31 = lane & 31u;
  auto nohook = [&](auto) {};
  auto run = [&](auto e, v4u srd, u32 lds, u32 mem) { wdma(IC<e.v & 3>{}, srd, lds + (e.v >> 2) * 4096u, mem + (e.v >> 2) * 32768u); };
  auto hk_wo = [&](auto e, u32 slot, u32 mem) { run(e, srdWo, slot + r * 8192u, mem + r * 65536u); };
  auto hk_s = [&](auto e, u32 slot) { run(e, srdW2, slot + r * 8192u, slot * 8u + r * 65536u); };
  constexpr u32 PA = 2u, PB = 1u, PC = 0u;
  auto hk_p = [&](auto e) { hk_s(IC<e.v & 7>{}, SLOT(e.v < 16 ? 2 + (e.v >> 3) : 3 - (e.v >> 3))); };
  auto hk_hi = [&](auto e) { run(e, srdW2, 32u * 4096u + r * 6144u, 37u * 32768u + r * 49152u); };
  auto hk_lo = [&](auto e) { run(e, srdW2, 27u * 4096u + r * 5120u, 32u * 32768u + r * 40960u); };
  auto mark = [&](u32 a) { *(volatile LAS unsigned char*)(a + r) = 1; };
  auto put_flag = [&](u32 a) { __builtin_amdgcn_raw_buffer_store_b64((v2u){tag, tag2}, rsW, l16v << 20, a, 16); };
  auto put_line = [&](u32 a) {
    v4u f = {tag, tag2, tag, tag2};
    OPQ(f);
    __builtin_amdgcn_raw_buffer_store_b128(f, rsW, (l16v & 127u) | ((l16v >> 7) << 24), a, 16);
    hold(f, f);
  };
  v2u fl;
  auto flags_ok = [&](u32 voff) {
    OPQ(fl);
    if (RARE(fl.x != tag || fl.y != tag2)) { wait_flags(rsW, voff, tag, tag2); }
  };
  auto p1_end = [&](auto&& hook) {
    FENCE();
    acc0 = mfma32(frag(2, 0), xr[30], acc0);
    FENCE();
    const f4 ka = *(LAS f4*)(csr), kb = *(LAS f4*)(csr + 16u);
    hook(IC<0>{});
    FENCE();
    acc0 = mfma32(frag(3, 0), xr[31], acc0);
    hook(IC<1>{});
    acc1 = mfma32(frag(2, 1), xr[30], acc1);
    FENCE();
    v4u fa = fold_w(ka, ISM ? aphi[0][0] : 0u);
    OPQ(fa);
    hook(IC<2>{});
    FENCE();
    acc0 = mfma32(fa, fx, acc0);
    FENCE();
    v4u fb = fold_w(kb, ISM ? aphi[0][1] : 0u);
    OPQ(fb);
    hook(IC<3>{});
    FENCE();
    acc1 = mfma32(frag(3, 1), xr[31], acc1);
    hook(IC<4>{});
    acc1 = mfma32(fb, fx, acc1);
    hook(IC<5>{});
    FENCE();
  };
  bar_all<4>();
  OPQ(pd0);
  PRIO(2 - hf);
  f16v acc2;
  f4 cs2 = {0.f, 0.f, 0.f, 0.f};
#pragma unroll
  for (int i = 0; i < 16; ++i) acc2[i] = 0.f;
  auto g2 = [&](auto&& pre, auto&& hook) {
    v4u u[32];
    auto rd = [&](int i) { u[i] = ldsfrag(wa[i & 3] + SLOT(i >> 4) + ((i >> 2) & 3) * 1024u, 512u); };
    sfor<0, GD>([&](auto i) { rd(i.v); });
    pre();
    sfor<0, 32>([&](auto ic) {
      constexpr int i = ic.v;
      if constexpr (i + GD < 32) rd(i + GD);
      FENCE();
      acc2 = mfma32(u[i], xr[i], acc2);
      FENCE();
      if constexpr (i == 0) cs2 = mfma16(sel, u[0], cs2);
      if constexpr ((i & 3) == 2 && i < 30) cs2 = mfma16(sel, u[i + 2], cs2);
      if constexpr (i == 28) *(LAS float*)(csw + 32u) = cs2[0];
      if constexpr (i == (hf && !ISM ? 30 : 28)) { if constexpr (hf && !ISM) wait_vm<7>(); mark(SY_G + hf * 4u); }
      hook(ic);
    });
  };
  auto fold2 = [&](u32 bb) { acc2 = mfma32(fold_w(*(LAS f4*)(csr + 32u), bb), fx, acc2); };
  v4u wr[32];
  u32 pa1;
  auto to_reg = [&](u32 i, u32 off) { FENCE(); if (i == 0) pa1 = opq(pa0 ^ 256u); wr[i] = ldsfrag((i & 1u ? pa1 : pa0) + (off + (i >> 1) * 2048u), 1024u); FENCE(); };
  u32 si;
  auto meet4 = [&](auto i, auto at, u32 a) {
    if constexpr (i.v == at.v - 1) si = lds_peek4(a);
    if constexpr (i.v == at.v) lds_wait<4>(a, si);
  };
  auto wo_reg = [&](auto j) { to_reg(j.v, j.v < 16 ? 32768u : -16384u); };
  auto g2_first = [&]() {
    g2([&] { p1_end(nohook); }, [&](auto i) {
      if constexpr (i.v & 1) hk_wo(IC<(i.v / 2) & 7>{}, SLOT(i.v < 16 ? 1 : 0), i.v < 16 ? 0u : 262144u);
      if constexpr (i.v == 12 - GD) { wait_vm<(13 - GD) / 2>(); mark(SY_U + 4u); }
      if constexpr (i.v == 14 - GD) si = lds_peek8(SY_U);
      if constexpr (i.v == 15 - GD) { lds_wait<8>(SY_U, si); }
    });
  };
  auto g2_hk = [&](auto i) {
    meet4(i, IC<15 - GD>{}, SY_G + 4u);
    if constexpr (i.v == 15 - GD) mark(SY_H);
    meet4(i, IC<14>{}, SY_H);
    if constexpr (i.v >= 14 && i.v < 22) hk_p(IC<i.v - 14>{});
    if constexpr (!ISM) {
      meet4(i, IC<22>{}, SY_A);
      if constexpr (i.v >= 22 && i.v < 30) hk_p(IC<i.v - 6>{});
    }
  };
  auto over7 = [&](auto q, auto n, auto&& f) { if constexpr (q.v > 0) sfor<((q.v - 1) * n.v + 6) / 7, (q.v * n.v + 6) / 7>(f); };
  auto g2_end = [&]() {
    spin_lds(SY_G);
  };
  auto lo_part = [&]() { spin_lds(SY_TF); sfor<0, 5>(hk_lo); };
  auto st16 = [&](v4u v, u32 voff, u32 soff = 0u) { __builtin_amdgcn_raw_buffer_store_b128(v, rsW, voff, soff, 16); };
  auto st32 = [&](v4u a, v4u c, u32 voff, u32 soff) { st16(a, voff, soff); st16(c, voff + 1024u, soff); hold(a, c); };
  const u32 tk = 2u * tb + mrow;
  const u32 fh = WS_FH + (tok32 * 16u + idx * 2u + hf) * FSTR;
  u32 vfe;
  const u32 hs = WS_H + tk * 86u * 1024u;
  const u32 kA = PA * 16u + kh * 8u, kB = PB * 16u + kh * 8u, kC = PC * 16u + kh * 8u, kD = 48u + kh * 14u;
  const u32 hsT = hs + (54u + kh * 5u) * 1024u, hsA = hs + kA * 1024u, hsB = hs + kB * 1024u, hsC = hs + kC * 1024u;
  const u32 hsD = hs + (48u + kh * 24u) * 1024u, hsE = hs + (64u + kh * 14u) * 1024u;
  auto ldact = [&](int i) {
    const int j = i < 5 ? i : i < 35 ? (i - 5) & 7 : i - 35;
    return __builtin_amdgcn_raw_buffer_load_b128(rsW, l16v + (j & 3) * 1024, (i < 5 ? hsT : i < 13 ? hsA : i < 21 ? hsB : i < 29 ? hsC : i < 35 ? hsD : hsE) + (j >> 2) * 4096, 16);
  };
  v4u act[43], xa, xb;
  u32 xo;
  const u32 xs = tk * 32768u + nb * 128u + kh * 64u;
  auto ldx = [&]() {
    const __amdgpu_buffer_rsrc_t rsX = __builtin_amdgcn_make_buffer_rsrc((void*)px, 0, 2097152, 0x00020000);
    FENCE();
    xo = opq((lane >> 2) * 1024u + (lane & 3u) * 16u);
    xa = __builtin_amdgcn_raw_buffer_load_b128(rsX, xo, xs, 0); xb = __builtin_amdgcn_raw_buffer_load_b128(rsX, xo, xs + 16384u, 0);
    FENCE();
  };
  v2u f2l;
  u32 vfl;
  auto late_flags = [&]() {
    vfl = opq(WS_FL + (tk * 16u + ((lane & 15u) | ((lane & 15u) > 5u))) * FSTR);
    f2l = __builtin_amdgcn_raw_buffer_load_b64(rsW, vfl, 0, 16);
  };
  const u32 fob = WS_FH + (tok32 * 16u + idx * 2u) * FSTR;
  auto act_first = [&]() { spin_flags(fl, srdS, vfe, ((u64)tag2 << 32) | tag); };

  f16v d0, d1;
#pragma unroll
  for (int i = 0; i < 16; ++i) { d0[i] = 0.f; d1[i] = 0.f; }
  auto unpack = [&](f16v& d, v4u a, v4u c) {
#pragma unroll
    for (int j = 0; j < 4; ++j) { d[2 * j] = blo(a[j]); d[2 * j + 1] = bhi(a[j]); d[8 + 2 * j] = blo(c[j]); d[9 + 2 * j] = bhi(c[j]); }
    OPQ(d);
  };
  auto bias = [&]() { const u32 a = opq(SM_B2 + kh * 64u + hh * 16u); unpack(d0, lds16(a), lds16(a + 32u)); };

  if constexpr (ISM) {
    const float c0 = -2.302208198f * rinv;
    const float c1 = -0.1029432397f * rinv * rinv * rinv;
    const u32 bC = aphi[0][2];
    const u32 hb = WS_H + tok32 * 86u * 1024u;
    const u32 so0 = hb + (4u * idx + hf) * 2048u, so1 = so0 + 4096u, so2 = hb + (seg2 + (six ? hf : 0u)) * 2048u;
    v4u o0, o1;
    if constexpr (hf) {
      g2_first();
      PRIO(1);
      gelu16(acc0, c0, c1, sig, o0, o1, [&](auto q) { meet4(q, IC<7>{}, SY_G + 4u); });
      st32(o0, o1, l16v, so0);
      fold2(bC);
      gelu16(acc1, c0, c1, sig, o0, o1, [&](auto q) { if constexpr (q.v == 6) { wait_vm<2>(); mark(SY_A); } });
      st32(o0, o1, l16v, so1);
      gelu16(acc2, c0, c1, sig, o0, o1, [&](auto q) {
        meet4(q, IC<1>{}, SY_A);
        over7(q, IC<32>{}, wo_reg);
        if constexpr (q.v == 4) { wait_vm<0>(); put_line(fh); }
      });
      mark(SY_I + 4u);
      st32(o0, o1, l16v, so2);
      PRIO(0);
      spin_lds(SY_I + 4u);
      sfor<16, 20>(hk_p);
      wait_vm<4>();
      put_line(fh + 1024u * FSTR);
      sfor<20, 32>(hk_p);
      wait_vm<8>();
      mark(SY_W + 4u);
      if (!six) wait_vm<4>();
      mark(SY_KV + 4u);
      ldx();
      lo_part();
      wait_vm<5>();
      mark(SY_W + 8u);
    } else {
      put_flag((WS_FC + (t * 8u + idx) * 8u) | (r ? 0x80000000u : 0u));
      v4u ff = {~0u, ~0u, ~0u, ~0u};
      OPQ(ff);
      auto fill = [&](auto e) { FENCE(); st16(ff, l16v + e.v * 1024u, WS_O + (tok32 * 32u + idx * 4u) * 1024u); FENCE(); };
      p1_end([&](auto e) { if constexpr (e.v < 4) fill(e); });
      OPQ(ff);
      PRIO(0);
      gelu16(acc0, c0, c1, sig, o0, o1, [&](auto q) { if constexpr (q.v == 2) { wait_vm<5>(); mark(SY_U); } });
      st32(o0, o1, l16v, so0);
      gelu16(acc1, c0, c1, sig, o0, o1, nohook);
      st32(o0, o1, l16v, so1);
      auto paced = [&](auto e) { hk_p(e); asm volatile("s_sleep 1"); };
      if (six) {
        PRIO(2);
        g2([] {}, [&](auto i) {
          if constexpr (i.v < 12 && (i.v & 1)) hk_hi(IC<i.v / 2>{});
          if constexpr (i.v == 16) { wait_vm<8>(); put_line(fh); }
          g2_hk(i);
        });
        g2_end();
        fold2(bC);
        PRIO(0);
        gelu16(acc2, c0, c1, sig, o0, o1, nohook);
        st32(o0, o1, l16v, so2);
        PRIO(0);
      } else {
        sfor<0, 6>(hk_hi);
        wait_vm<6>();
        put_line(fh);
        spin_lds(SY_G + 4u);
        sfor<0, 8>(paced);
      }
      vfe = opq(WS_FH + (tk * 16u + (lane & 15u)) * FSTR);
      sfor<8, 12>(paced);
      if (six) { wait_vm<4>(); put_line(fh + 1024u * FSTR); }
      sfor<12, 16>(paced);
      spin_lds(SY_KV + 4u);
      ld8_sc1(fl, srdS, vfe);
      wait_vm<0>();
      lds_arrive(SY_KV + r);
    }
  } else {
    const float cl = rinv * 1.4426950409f;
    f16v zero;
#pragma unroll
    for (int i = 0; i < 16; ++i) zero[i] = 0.f;
    v4u ph[4], vv[2];
    f16v pp[2];
    auto logits = [&](v4u k0, v4u k1) {
      pp[0] = mfma32(aphi[0], k0, zero); pp[1] = mfma32(aphi[1], k1, zero);
      FENCE();
    };
    auto put16 = [&](u32 a, v4u v) { FENCE(); *(LAS v4u*)a = v; FENCE(); };
    auto scale_v = [&]() {
#pragma unroll
      for (int i = 0; i < 8; ++i) vv[i >> 2][i & 3] = pk(mul1(acc1[2 * i], rinv), mul1(acc1[2 * i + 1], rinv));
      asm volatile("" : "+v"(vv[0]), "+v"(vv[1]));
      FENCE();
    };
    v4u fa[4], fb[4];
    auto kv_read = [&](auto j) { if constexpr (j.v & 1) fb[j.v >> 1] = ldsfrag(ad[6] + (j.v >> 1) * 1024u, 256u); else fa[j.v >> 1] = ldsfrag(ad[5] + (j.v >> 1) * 1024u, 256u); };
    f4 akv = {0.f, 0.f, 0.f, 0.f}, az = {0.f, 0.f, 0.f, 0.f};
    auto kv_sum = [&](auto k) {
      const v4u ones = {one2, one2, one2, one2};
      FENCE();
      az = mfma16(fa[k.v], ones, az); akv = mfma16(fa[k.v], fb[k.v], akv);
      FENCE();
    };
    auto kv_put = [&]() {
      FENCE();
      __builtin_amdgcn_raw_buffer_store_b64((v2u){pk(akv[0], akv[1]), pk(akv[2], akv[3])}, rsW, ad[0], WS_KVP + pi * 512u, 16);
      __builtin_amdgcn_raw_buffer_store_b64((v2u){pk(az[0], az[1]), pk(az[2], az[3])}, rsW, ad[1], WS_ZP + pi * 32u, 16);
      FENCE();
    };
    v4u pv[9];
    u32 gv, gz;
    const u32 gs0 = WS_KVP + (idx * 8u + un) * 8192u, gs1 = gs0 + 4096u, gsz = WS_ZP + (idx * 8u + un) * 512u;
    auto gather_adr = [&]() {
      gv = opq((lane >> 4) * 512u + (lane & 15u) * 16u);
      gz = opq(((lane & 6u) * 2u + (lane >> 4)) * 32u + (lane & 1u) * 16u);
    };
    auto gather_one = [&](auto j) {
      FENCE();
      constexpr int i = j.v - 1;
      if constexpr (j.v == 0) ld16o<0>(pv[8], srdS, gz, gsz);
      else ld16o<(i & 1) * 256 + (i & 2) * 1024>(pv[i], srdS, gv, i < 4 ? gs0 : gs1);
      FENCE();
    };
    v4u sa[2];
    u32 a0, a1;
    auto gather_sel = [&](auto j) {
      const u32 m = lane & 15u, kq = lane >> 4, one = (m & 1u) ? 0x3f800000u : 0x3f80u;
      if constexpr (j.v < 2) {
#pragma unroll
        for (u32 i = 0; i < 4; ++i) sa[j.v][i] = (m >> 1) == i + 4u * j.v ? one : 0u;
        pin(sa[j.v]);
      }
      const u32 c = 2u * (m & 7u) + (kq & 1u), d = (c & 3u) + 8u * ((c >> 2) & 1u) + 4u * (c >> 3);
      if constexpr (j.v == 2) a0 = opq(SM_KVT + un * 544u + d * 32u + ((kq & 2u) + (m >> 3)) * 8u);
      if constexpr (j.v == 3) a1 = opq(kq < 2u ? SM_KVT + un * 544u + 512u + (m & 1u) * 16u + kq * 8u : SM_DUMP);
    };
    auto gather_sum = [&](auto nv) {
      f4 s, z;
      auto sum = [&](auto n0) {
        s = (f4){0.f, 0.f, 0.f, 0.f};
        wait_vm<n0.v < 0 ? 0 : n0.v + 4>();
        pin4(pv[0], pv[1], pv[2], pv[3]); pin(pv[8]);
        z = mfma16(sa[0], pv[8], s);
#pragma unroll
        for (int i = 0; i < 4; ++i) s = mfma16(sa[i & 1], pv[i], s);
#pragma unroll
        for (int j = 0; j < 4; ++j) {
          z[j] += __uint_as_float(__builtin_amdgcn_update_dpp(0u, __float_as_uint(z[j]), 0x122, 0xf, 0xf, false));
          z[j] += __uint_as_float(__builtin_amdgcn_update_dpp(0u, __float_as_uint(z[j]), 0x124, 0xf, 0xf, false));
        }
        pin(z);
        wait_vm<n0.v < 0 ? 0 : n0.v>();
        pin4(pv[4], pv[5], pv[6], pv[7]);
#pragma unroll
        for (int i = 4; i < 8; ++i) s = mfma16(sa[i & 1], pv[i], s);
      };
      sum(nv);
      *(LAS v2u*)(a1) = (v2u){pk(z[0], z[1]), pk(z[2], z[3])};
      if (RARE(__builtin_isunordered(s[0], z[0]))) {
#pragma unroll 1
        for (int sp = 0; sp < SPIN_CAP; ++sp) {
          sfor<0, 9>(gather_one);
          sum(IC<-1>{});
          if (!__builtin_amdgcn_ballot_w64(__builtin_isunordered(s[0], z[0]))) break;
        }
        *(LAS v2u*)(a1) = (v2u){pk(z[0], z[1]), pk(z[2], z[3])};
      }
      *(LAS v2u*)(a0) = (v2u){pk(s[0], s[1]), pk(s[2], s[3])};
      mark(SY_KV + hf * 4u);
    };
    auto put_o = [&](auto&& hook) {
      const u32 ka = opq(SM_KVT + (2u * hf + 4u * hh) * 544u + (l31 < 16u ? l31 : 16u) * 32u);
      spin_lds(SY_KV + hf * 4u);
      f16v d[2]; v4u ov[2];
      const v4u a0 = lds16(ka), a1 = lds16(ka + 544u), a2 = lds16(ka + 16u), a3 = lds16(ka + 560u);
      hook(IC<0>{});
      d[0] = mfma32(a0, ph[0], zero); d[0] = mfma32(a2, ph[1], d[0]);
      hook(IC<1>{});
      d[1] = mfma32(a1, ph[2], zero); d[1] = mfma32(a3, ph[3], d[1]);
      hook(IC<2>{});
#pragma unroll
      for (u32 e = 0; e < 2; ++e) {
        const float rd = __builtin_amdgcn_rcpf(d[e][8] + 1e-6f);
#pragma unroll
        for (int i = 0; i < 4; ++i) ov[e][i] = pk(mul1(d[e][2 * i], rd), mul1(d[e][2 * i + 1], rd));
      }
      st32(ov[0], ov[1], l16v, WS_O + (tok32 * 32u + idx * 4u + 2u * hf) * 1024u);
    };
    if constexpr (hf) {
      g2_first();
      PRIO(3);
      logits(pk8(acc0, 0), pk8(acc0, 8));
      PRIO(0);
      scale_v();
      spin_lds(SY_G + 4u);
      PRIO(3);
      fold2(0u);
      PRIO(1);
      v4u qa, qb;
      auto wo2 = [&](auto q, auto h) { wo_reg(IC<16 * h.v + 2 * q.v>{}); wo_reg(IC<16 * h.v + 2 * q.v + 1>{}); };
      softmax16<false>(pp[0], max16(pp[0]), cl, ph[0], ph[1], [&](auto q) {
        if constexpr (q.v == 1) { qa = pk8(acc2, 0); qb = pk8(acc2, 8); pin(qa); pin(qb); }
        if constexpr (q.v < 4) { wo2(IC<2 * q.v>{}, IC<0>{}); wo2(IC<2 * q.v + 1>{}, IC<0>{}); }
        if constexpr (q.v == 3) mark(SY_A);
      });
      ld8_sc1(fl, srdS, ad[7]);
      softmax16<false>(pp[1], max16(pp[1]), cl, ph[2], ph[3], [&](auto q) {
        meet4(q, IC<2>{}, SY_IM);
        if constexpr (q.v == 2) { put16(ad[4], vv[0]); put16(ad[4] + 4096u, vv[1]); }
        if constexpr (q.v == 3) { put16(ad[2], ph[0]); put16(ad[3], ph[1]); }
        if constexpr (q.v == 6) put16(ad[2] + 8192u, ph[2]);
      });
      put16(ad[3] + 8192u, ph[3]);
      wait_vm<1>();
      lds_arrive(SY_I + 4u + r);
      PRIO(3);
      logits(qa, qb);
      gather_adr();
      spin_lds(SY_I + 4u);
      sfor<0, 8>(kv_read);
      mark(SY_IM + 4u);
      float mx0 = max16(pp[0]), mx1 = max16(pp[1]);
      pin(mx0); pin(mx1);
      sfor<0, 4>(kv_sum);
      const u32 vfo = opq(fob);
      kv_put();
      PRIO(0);
      softmax16<true>(pp[0], mx0, cl, ph[0], ph[1], [&](auto q) { wo2(q, IC<1>{}); });
      mark(SY_B);
      wait_vm<2>();
      flags_ok(ad[7]);
      softmax16<true>(pp[1], mx1, cl, ph[2], ph[3], nohook);
      ldx();
      sfor<0, 4>(gather_sel);
      sfor<0, 9>(gather_one);
      ld8_sc1(fl, srdS, vfo);
      PRIO(2);
      gather_sum(IC<1>{});
      wait_vm<0>();
      flags_ok(vfo);
      put_o(nohook);
      lo_part();
    } else {
      put_flag((WS_FC + 1024u + (idx * 16u + t) * 8u) | (r ? 0x80000000u : 0u));
      p1_end(nohook);
      logits(pk8(acc0, 0), pk8(acc0, 8));
      PRIO(0);
      scale_v();
      put16(ad[4], vv[0]); put16(ad[4] + 4096u, vv[1]);
      softmax16<false>(pp[0], max16(pp[0]), cl, ph[0], ph[1], [&](auto q) {
        if constexpr (q.v == 1) { wait_vm<1>(); mark(SY_U); }
        if constexpr (q.v == 6) put16(ad[2], ph[0]);
      });
      put16(ad[3], ph[1]);
      softmax16<false>(pp[1], max16(pp[1]), cl, ph[2], ph[3], [&](auto q) { if constexpr (q.v == 6) put16(ad[2] + 8192u, ph[2]); });
      put16(ad[3] + 8192u, ph[3]);
      lds_arrive(SY_I + r);
      PRIO(2);
      g2([] {}, [&](auto i) {
        meet4(i, IC<2>{}, SY_I);
        if constexpr (i.v >= 2 && i.v < 6) { kv_read(IC<2 * i.v - 4>{}); kv_read(IC<2 * i.v - 3>{}); }
        if constexpr (i.v == 5) { mark(SY_IM); }
        if constexpr (i.v >= 5 && i.v < 9) kv_sum(IC<i.v - 5>{});
        if constexpr (i.v == 10) kv_put();
        g2_hk(i);
        if constexpr (i.v >= 16 && i.v < 24 && !(i.v & 1)) gather_sel(IC<(i.v - 16) / 2>{});
        if constexpr (i.v == 24) gather_adr();
        if constexpr (i.v == 26) vfe = opq(lane < 16u ? WS_FH + (tk * 16u + lane) * FSTR : fob);
        if constexpr (i.v == 13) { FENCE(); ld8_sc1(fl, srdS, ad[7]); FENCE(); }
        if constexpr (i.v == 30) { wait_vm<16>(); flags_ok(ad[7]); }
      });
      g2_end();
      auto hk_q = [&](auto e) {
        if constexpr (e.v == 15) si = lds_peek4(SY_IM + 4u);
        if constexpr (e.v == 17) lds_wait<4>(SY_IM + 4u, si);
        if constexpr (e.v < 8) hk_p(IC<8 + e.v>{});
        else if constexpr (e.v < 17) gather_one(IC<e.v - 8>{});
        else hk_hi(IC<e.v - 17>{});
      };
      hk_q(IC<0>{});
      fold2(0u);
      hk_q(IC<1>{});
      logits(pk8(acc2, 0), pk8(acc2, 8));
      hk_q(IC<2>{});
      PRIO(0);
      softmax16<true>(pp[0], max16(pp[0]), cl, ph[0], ph[1], [&](auto q) { sfor<3 + (q.v * 11 + 7) / 8, 3 + ((q.v + 1) * 11 + 7) / 8>(hk_q); });
      hk_q(IC<14>{});
      softmax16<true>(pp[1], max16(pp[1]), cl, ph[2], ph[3], [&](auto q) { hk_q(IC<15 + q.v>{}); });
      gather_sum(IC<6>{});
      ld8_sc1(fl, srdS, vfe);
      spin_lds(SY_B);
      sfor<24, 32>([&](auto e) { hk_p(e); if constexpr (e.v == 25) bias(); });
      wait_vm<8>();
      act_first();
      sfor<0, MWIN - 3>([&](auto q) { FENCE(); act[q.v] = ldact(q.v); FENCE(); });
      put_o([&](auto q) {
        FENCE(); act[MWIN - 3 + q.v] = ldact(MWIN - 3 + q.v); FENCE();
        if constexpr (q.v > 0) sfor<0, 5>([&](auto i) { to_reg(5 * q.v - 5 + i.v, 0u); });
        if constexpr (q.v == 2) mark(SY_TF);
      });
    }
  }

  auto put4 = [&](u32 a, const f16v& d, int g) { *(LAS f4*)(a + g * 1024u) = (f4){d[4 * g], d[4 * g + 1], d[4 * g + 2], d[4 * g + 3]}; };
  u32 ea, eb;
  f4 pt[4];
  u32 sy;
  if constexpr (!hf) {
    constexpr int WIN = MWIN;
    if constexpr (ISM) act_first();
    PRIO(1);
#pragma unroll
    for (int i = ISM ? 0 : WIN; i < WIN; ++i) act[i] = ldact(i);
    FENCE();
    if constexpr (ISM) { spin_lds(SY_KV); sfor<0, 10>([&](auto i) { to_reg(i.v, 0u); }); mark(SY_TF); }
    const u32 pab = pa0 - (27u * 4096u + kh * 10240u);
    u32 la[2];
    if constexpr (ISM) bias();
    FENCE();
    v4u nf[43][2];
    auto rdw = [&](int i, int h) {
      const int j = i < 29 ? (i - 5) & 7 : i - 29;
      if (i >= 5 && j == 0) la[h] = opq(h ? la[0] ^ 256u : pab + (i < 13 ? kA : i < 21 ? kB : i < 29 ? kC : kD) * 2048u);
      if (i >= 5 && i < 43) nf[i][h] = ldsfrag(la[h] + j * 2048u, 1024u);
    };
    constexpr int SF = ISM ? 5 : 4;
    auto upto = [](int i) { const int m = i <= WIN + SF ? 29 : 43; return WIN + i < m ? WIN + i : m; };
    auto stp = [](int n) { return n < 86 - 2 * NE ? n / 2 : n < 86 - NE ? n - (43 - NE) : n - 43; };
    auto hlf = [](int n) { return n < 86 - 2 * NE ? n & 1 : n < 86 - NE; };
    u32 fa;
    auto prod = [&](auto nc) {
      constexpr int n = nc.v, k = stp(n), h = hlf(n), g = n - (87 - NE);
      f16v& d = h ? d1 : d0;
      if constexpr (k < 5) d = mfma32(wr[2 * k + h], act[k], d); else if constexpr (n < 86) d = mfma32(nf[k][h], act[k], d);
      FENCE();
      if constexpr (g == -4) { ea = opq(l16v + r * 4096u); eb = opq(ea ^ 8192u); fa = opq(SY_X + (r ^ 2u)); }
      if constexpr (g == -3) sy = lds_peek4(SY_FREE);
      if constexpr (g == -1) lds_wait<4>(SY_FREE, sy);
      if constexpr (g >= 0 && g < 4) put4(ea, d1, g);
      if constexpr (g == 3) mark(SY_X);
    };
    auto rdn = [&](int n) { if (n < 86) rdw(stp(n), hlf(n)); };
    sfor<0, 43>([&](auto ic) {
      constexpr int i = ic.v;
      prod(IC<2 * i>{});
      constexpr u32 sa = SY_W + (i >> 3) * 4;
      constexpr bool mt = i < 29 && i > 7 && (ISM || i >> 3 != 2);
      if constexpr (!ISM && i == 0) mark(SY_W + 4u);
      if constexpr (mt && (i & 7) == 5 - MD) lds_wait<4>(sa, sy);
      rdn(2 * (i + MD));
      if constexpr (mt && (i & 7) == 4 - MD) sy = lds_peek4(sa);
      FENCE();
      if constexpr (i == SF) late_flags();
      if constexpr (upto(i) <= 29 && upto(i + 1) > 29) { if (RARE(f2l.x != tag || f2l.y != tag2)) { wait_flags(rsW, vfl, tag, tag2); } }
#pragma unroll
      for (int j = upto(i); j < upto(i + 1); ++j) act[j] = ldact(j);
      if constexpr (ISM && i == 27) mark(SY_W);
      if constexpr (i == 29) *(volatile LAS unsigned char*)(SY_FREE + r) = 1;
      FENCE();
      prod(IC<2 * i + 1>{});
      rdn(2 * (i + MD) + 1);
      FENCE();
    });
    PRIO(3);
    sfor<86, 91>(prod);
    spin_get<0>(fa, eb, pt);
    sfor<0, 2>([&](auto gc) {
      float m[8], e[8];
#pragma unroll
      for (int j = 0; j < 8; ++j) { m[j] = d0[8 * gc.v + j] + pt[2 * gc.v + (j >> 2)][j & 3]; OPQ(m[j]); }
#pragma unroll
      for (int j = 0; j < 8; ++j) { e[j] = __builtin_amdgcn_exp2f(mul1(m[j], -1.4426950409f)); OPQ(e[j]); }
#pragma unroll
      for (int j = 0; j < 8; ++j) { e[j] += 1.f; OPQ(e[j]); }
#pragma unroll
      for (int j = 0; j < 8; ++j) { e[j] = __builtin_amdgcn_rcpf(e[j]); OPQ(e[j]); }
#pragma unroll
      for (int j = 0; j < 8; ++j) d0[8 * gc.v + j] = mul1(m[j], e[j]);
      put4(ea + 16384u, d0, 2 * gc.v); put4(ea + 16384u, d0, 2 * gc.v + 1);
      FENCE();
    });
    mark(SY_Y);
    asm volatile("s_waitcnt lgkmcnt(0)" ::: "memory");
  } else {
    PRIO(0);
    const u32 ov = WS_O + (tk * 32u + kh * 8u) * 1024u;
    const u32 vq = opq((lane & 1u) * 4096u + (lane & 2u) * 8192u + 2044u);
    v4u ao[2][8];
    auto ldo = [&](u32 ph, u32 i) { ao[ph][i] = __builtin_amdgcn_raw_buffer_load_b128(rsW, l16v + (ph * 2u + (i & 1u)) * 1024u, ov + (i & 2u) * 2048u + (i >> 2) * 16384u, 16); };
    auto fetch = [&](u32 ph) {
#pragma unroll
      for (u32 i = 0; i < 8; ++i) ldo(ph, i);
    };
    auto seen = [&](u32& mx, v4u a) { mx = max(max(mx, a[0]), a[1]); mx = max(max(mx, a[2]), a[3]); };
    auto again = [&](auto phc) {
      constexpr u32 ph = phc.v;
      spin_written<ph * 2048>(srdS, vq, ov);
#pragma unroll 1
      for (int sp = 0; sp < SPIN_CAP; ++sp) {
        asm volatile("" ::: "memory");
        fetch(ph);
        u32 mx = 0u;
#pragma unroll
        for (u32 i = 0; i < 8; ++i) seen(mx, ao[ph][i]);
        if (!__builtin_amdgcn_ballot_w64(mx == ~0u)) break;
      }
    };
    auto smul = [&](u32 ph, u32 i, u32 h, const f16v& c) { return mfma32(wr[(i >> 1) * 8u + ph * 4u + (i & 1u) * 2u + h], ao[ph][i], c); };
    constexpr u32 SO = 1, SB = 0, NB = 3;
    FENCE();
    const __amdgpu_buffer_rsrc_t rsO = __builtin_amdgcn_make_buffer_rsrc((void*)pout, 0, 2097152, 0x00020000);
    wait_vm<0>();
    mark(SY_W + 12u);
    if constexpr (!ISM) { sfor<0, 8>([&](auto i) { ldo(SO, i.v); }); FENCE(); }
    if constexpr (ISM) {
      if (b == 255u && w == 7u) {
#pragma unroll
        for (u32 i = 0; i < 4; ++i) wait_flags(rsW, WS_FC + i * 512u + lane * 8u, tag, tag2);
        __builtin_amdgcn_raw_buffer_store_b32(tag, rsW, l16v << 20, WS_E, 16);
      }
      spin_lds(SY_W);
      fetch(SO);
      FENCE();
    }
    const f16v z16 = d0;
    u32 mx = 0u;
    sfor<0, NB>([&](auto i) { ldo(SB, i.v); });
    sfor<0, 8>([&](auto i) {
      FENCE();
      d0 = smul(SO, i.v, 0, d0);
      FENCE();
      seen(mx, ao[SO][i.v]);
      OPQ(mx);
      FENCE();
      d1 = smul(SO, i.v, 1, d1);
      FENCE();
      if constexpr (NB + i.v < 8) ldo(SB, NB + i.v);
    });
    if (RARE(mx == ~0u)) {
      again(IC<SO>{});
      d0 = z16; d1 = z16;
#pragma unroll
      for (u32 i = 0; i < 8; ++i) { d0 = smul(SO, i, 0, d0); d1 = smul(SO, i, 1, d1); }
    }
    FENCE();
    PRIO(2);
    ea = opq(l16v + (r * 4096u + 16384u)); eb = opq(ea ^ 8192u);
    const u32 tr = opq((lane >> 2) * 80u + (lane & 3u) * 16u + (r * 4096u + 49152u)), tm = opq(l31 * 80u + hh * 16u + (r * 4096u + 49152u));
    spin_lds(SY_FREE);
    *(LAS v4u*)tr = xa; *(LAS v4u*)(tr + 1280u) = xb;
    const v4u xc = lds16(tm), xd = lds16(tm + 32u);
    f16v xf, e1;
    mx = 0u;
    sfor<0, 8>([&](auto i) {
      FENCE();
      e1 = smul(SB, i.v, 1, i.v ? e1 : d1);
      FENCE();
      seen(mx, ao[SB][i.v]);
      OPQ(mx);
      xf[2 * i.v] = blo((i.v < 4 ? xc : xd)[i.v & 3]); xf[2 * i.v + 1] = bhi((i.v < 4 ? xc : xd)[i.v & 3]);
      OPQ(xf);
    });
    if (RARE(mx == ~0u)) {
      again(IC<SB>{});
      e1 = d1;
#pragma unroll
      for (u32 i = 0; i < 8; ++i) e1 = smul(SB, i, 1, e1);
    }
    f4 gm[4];
    u32 fa, fb;
    sfor<0, 8>([&](auto i) {
      FENCE();
      d0 = smul(SB, i.v, 0, d0);
      FENCE();
      constexpr int g = i.v - 1;
      if constexpr (g >= 0 && g < 4) put4(ea + 16384u, e1, g);
      if constexpr (g == 3) mark(SY_X + 4u);
      if constexpr (g == 4) { fa = opq(SY_X + 4u + (r ^ 2u)); fb = opq(SY_Y + r); }
    });
    FENCE();
    spin_get2<16384>(fa, fb, eb, ea, pt, gm);
#pragma unroll
    for (int i = 0; i < 16; ++i) d0[i] += pt[i >> 2][i & 3];
#pragma unroll
    for (int i = 0; i < 16; ++i) xf[i] = __builtin_fmaf(d0[i], gm[i >> 2][i & 3], xf[i]);
    *(LAS v4u*)tm = pk8(xf, 0); *(LAS v4u*)(tm + 32u) = pk8(xf, 8);
    const v4u oa = lds16(tr), ob = lds16(tr + 1280u);
    __builtin_amdgcn_raw_buffer_store_b128(oa, rsO, xo, xs, 17);
    __builtin_amdgcn_raw_buffer_store_b128(ob, rsO, xo, xs + 16384u, 17);
  }
  };
  auto finish = [&]() {
    asm volatile("" : "=v"(wf[8]), "=v"(wf[9]), "=v"(wf[10]), "=v"(wf[11]), "=v"(wf[12]), "=v"(wf[13]), "=v"(wf[14]), "=v"(wf[15]), "=v"(l16v), "=v"(pd0) :: "memory");
    asm volatile("" : "=v"(xr[0]), "=v"(xr[1]), "=v"(xr[2]), "=v"(xr[3]), "=v"(xr[4]), "=v"(xr[5]), "=v"(xr[6]), "=v"(xr[7]),
                 "=v"(xr[8]), "=v"(xr[9]), "=v"(xr[10]), "=v"(xr[11]), "=v"(xr[12]), "=v"(xr[13]), "=v"(xr[14]), "=v"(xr[15]), "=v"(acc0), "=v"(acc1),
                 "=v"(fx), "=v"(rinv), "=v"(sig), "=v"(vp), "=v"(aphi[0]), "=v"(aphi[1]), "=v"(sel), "=v"(csr), "=v"(csw), "=v"(pa0) :: "memory");
    asm volatile("" : "=v"(xr[16]), "=v"(xr[17]), "=v"(xr[18]), "=v"(xr[19]), "=v"(xr[20]), "=v"(xr[21]), "=v"(xr[22]), "=v"(xr[23]),
                 "=v"(xr[24]), "=v"(xr[25]), "=v"(xr[26]), "=v"(xr[27]), "=v"(xr[28]), "=v"(xr[29]), "=v"(xr[30]), "=v"(xr[31]), "=v"(tidv),
                 "=v"(wa[0]), "=v"(wa[1]), "=v"(wa[2]), "=v"(wa[3]), "=v"(ad[0]), "=v"(ad[1]), "=v"(ad[2]), "=v"(ad[3]), "=v"(ad[4]), "=v"(ad[5]), "=v"(ad[6]), "=v"(ad[7]) :: "memory");
  };
  u32 role = isM * 2u + hf;
  if (role == 0u) { tail(IC<0>{}, IC<0>{}); end_wave(); finish(); }
  asm volatile("" : "+s"(role));
  if (role == 1u) { tail(IC<0>{}, IC<1>{}); end_wave(); finish(); }
  asm volatile("" : "+s"(role));
  if (role == 2u) { tail(IC<1>{}, IC<0>{}); end_wave(); finish(); }
  tail(IC<1>{}, IC<1>{});
}

extern "C" void kernel_launch(void* const* d_in, const int* in_sizes, int n_in,
                              void* d_out, int out_size, void* d_ws, size_t ws_size,
                              hipStream_t stream) {
  Params p;
  p.x = (const char*)d_in[0]; p.wqkv = (const char*)d_in[1]; p.wphi = (const char*)d_in[2]; p.wo = (const char*)d_in[3];
  p.w1 = (const char*)d_in[5]; p.b1 = (const char*)d_in[6]; p.w2 = (const char*)d_in[7]; p.b2 = (const char*)d_in[8];
  p.out = (char*)d_out; p.ws = (char*)d_ws;
  (void)hipFuncSetAttribute((const void*)hh_fused, hipFuncAttributeMaxDynamicSharedMemorySize, (int)LDS_BYTES);
  hh_fused<<<256, 512, LDS_BYTES, stream>>>(p);
}
```
